```python
import torch

def _softmax(x, axis=-1):
    return torch.softmax(x, dim=axis)
def _gelu(x):
    return torch.nn.functional.gelu(x, approximate="tanh")
def _silu(x):
    return torch.nn.functional.silu(x)
def _sigmoid(x):
    return torch.sigmoid(x)
def _layernorm(x, axis=-1, eps=1e-5):
    m = x.mean(dim=axis, keepdim=True)
    v = x.var(dim=axis, keepdim=True, unbiased=False)
    return (x - m) / torch.sqrt(v + eps)
def _scatter_add(tgt, idx, src, axis=0):
    return tgt.clone().index_add_(axis, idx, src)
def _segment_sum(src, seg, n):
    out = torch.zeros((n,) + src.shape[1:], dtype=src.dtype)
    return out.index_add_(0, seg, src)
def _pad(x, pw):
    flat = tuple(v for pair in reversed(pw) for v in pair)
    return torch.nn.functional.pad(x, flat)

kernel_name = 'gather_scatter__kernel_systems_refs_featgraph__graph'

def setup_inputs():

    _w = (lambda a: (lambda t: t.to(torch.bfloat16) if t.is_floating_point() else t)(torch.as_tensor(a)))
    N, deg, D = 10000, 32, 128
    E = N * deg
    scale = D ** -0.5
    src = (lambda lo, hi, *s: torch.randint(lo, hi, s))(0, N, E).to(torch.int64)
    dst = (lambda lo, hi, *s: torch.randint(lo, hi, s))(0, N, E).to(torch.int64)
    return {
        "x": _w((lambda *s: torch.randn(*s, dtype=torch.bfloat16) * 0.8)(N, D)),
        "src": _w(src),
        "dst": _w(dst),
        "w_q": _w((lambda *s: torch.randn(*s, dtype=torch.bfloat16) * 0.8)(D, D) * scale),
        "w_v": _w((lambda *s: torch.randn(*s, dtype=torch.bfloat16) * 0.05)(D, D) * scale),
    }

def reference(x, src, dst, w_q, w_v):
    N, D = x.shape
    xs = x[src]
    xd = x[dst]
    q = xd @ w_q
    logits = torch.einsum('ed,ed->e', xs, q) * (float(D) ** -0.5)
    alpha = _sigmoid(logits)
    msg = alpha[:, None] * (xs @ w_v)
    return _segment_sum(msg, dst, N)
```

```cpp
#include <hip/hip_runtime.h>
#include <hip/hip_bf16.h>

typedef unsigned u32;
typedef u32 u32x2 __attribute__((ext_vector_type(2)));
typedef u32 u32x4 __attribute__((ext_vector_type(4)));
typedef int i32x4 __attribute__((ext_vector_type(4)));
typedef float f32x4 __attribute__((ext_vector_type(4)));
typedef short s16x4 __attribute__((ext_vector_type(4)));
typedef __bf16 bf16x8 __attribute__((ext_vector_type(8)));
typedef __bf16 bf16x2 __attribute__((ext_vector_type(2)));
typedef float f32x2 __attribute__((ext_vector_type(2)));
#define LAS __attribute__((address_space(3)))

constexpr int NN = 10000, NE = 320000, DD = 128;
constexpr int NB = 256;
constexpr int NPB = 40;
constexpr int NC = 128;
constexpr int CH = NE / NC;
constexpr int SLOT = 32;
constexpr int CAP = 96;
constexpr u32 EMPTY = 0x003FFF00u;
constexpr u32 OVF = 1u;
static_assert(CH % 4 == 0 && CH * NC == NE, "chunking");

constexpr int PITCH = 272;
constexpr int L_WQ = 0, L_WV = 32768, L_XD = 65536, L_QT = L_XD + 48 * PITCH, L_AG = L_QT + 48 * PITCH,
              L_AL = L_AG + 48 * PITCH, L_Z = L_AL + 16 * 512, L_CNT = L_Z + 64, L_FLAG = L_CNT + 256,
              L_LIST = L_FLAG + 64, L_END = L_LIST + NPB * CAP * 4 + NC * SLOT * 4;

#define STAMP(i) do {} while (0)

__device__ __forceinline__ u32 pk_bf16(float lo, float hi) {
  f32x2 v = {lo, hi};
  bf16x2 b = __builtin_convertvector(v, bf16x2);
  return __builtin_bit_cast(u32, b);
}
__device__ __forceinline__ f32x4 mfma32(u32x4 a, u32x4 b, f32x4 c) {
  return __builtin_amdgcn_mfma_f32_16x16x32_bf16(__builtin_bit_cast(bf16x8, a), __builtin_bit_cast(bf16x8, b), c, 0, 0, 0);
}
__device__ __forceinline__ f32x4 mfma4(u32 a0, u32 a1, u32 b0, u32 b1, f32x4 c) {
  u32x2 a = {a0, a1}, b = {b0, b1};
  return __builtin_amdgcn_mfma_f32_4x4x4bf16_1k(__builtin_bit_cast(s16x4, a), __builtin_bit_cast(s16x4, b), c, 0, 0, 0);
}
template <int CTRL, int BANK> __device__ __forceinline__ float dpp(float old, float v) {
  return __builtin_bit_cast(float, __builtin_amdgcn_update_dpp(__builtin_bit_cast(int, old), __builtin_bit_cast(int, v), CTRL, 0xF, BANK, false));
}
template <int CTRL> __device__ __forceinline__ float dppa(float v) {
  return __builtin_bit_cast(float, __builtin_amdgcn_update_dpp(0, __builtin_bit_cast(int, v), CTRL, 0xF, 0xF, true));
}

__global__ __launch_bounds__(1024) void bin_kernel(const int* __restrict__ src, const int* __restrict__ dst,
                                                   const char* __restrict__ x, u32* __restrict__ slots, char* stamps) {
  __shared__ __attribute__((aligned(16))) u32 img[NB * SLOT];
  __shared__ u32 cnt[NB];
  const int tid = threadIdx.x, c = blockIdx.x;
  STAMP(0);
  if (c >= NC) {
    const int part = (c - NC) >> 3;
    const int line = part * 1250 + tid;
    u32 a = *(const u32*)(x + (size_t)line * 128);
    asm volatile("" :: "v"(a));
    if (tid < 226) { u32 b = *(const u32*)(x + (size_t)(line + 1024) * 128); asm volatile("" :: "v"(b)); }
    STAMP(3);
    return;
  }
  i32x4 d4 = {0, 0, 0, 0}, s4 = {0, 0, 0, 0};
  const bool act = tid < CH / 4;
  if (act) { d4 = ((const i32x4*)dst)[c * (CH / 4) + tid]; s4 = ((const i32x4*)src)[c * (CH / 4) + tid]; }
  const u32x4 e4 = {EMPTY, EMPTY, EMPTY, EMPTY};
  ((u32x4*)img)[tid] = e4; ((u32x4*)img)[tid + 1024] = e4;
  if (tid < NB) cnt[tid] = 0;
  __syncthreads();
  if (act) {
    u32 r[4];
#pragma unroll
    for (int j = 0; j < 4; ++j) r[j] = atomicAdd(&cnt[d4[j] & 255], 1u);
#pragma unroll
    for (int j = 0; j < 4; ++j) {
      const u32 b = d4[j] & 255;
      if (r[j] < SLOT) img[b * SLOT + ((r[j] + b) & (SLOT - 1))] = ((u32)s4[j] << 8) | (((u32)d4[j] >> 8) << 2);
    }
  }
  STAMP(1);
  __syncthreads();
#pragma unroll
  for (int k = 0; k < 2; ++k) {
    const int i = tid + k * 1024, b = i >> 3, p = i & 7;
    u32x4 v = ((const u32x4*)img)[i];
    if (cnt[b] > SLOT) v.x |= OVF;
    ((u32x4*)slots)[(b * NC + c) * 8 + p] = v;
  }
  STAMP(2);
}

__device__ __forceinline__ void proj_tile(char LAS* lds, int woff, int rowoff, int cb, int lane, f32x4& o0, f32x4& o1) {
  const int g = lane >> 4, q = (lane >> 2) & 3, p = lane & 3;
  o0 = (f32x4){0, 0, 0, 0}; o1 = o0;
  const int wa = woff + (8 * g + q) * 256 + (((4 * cb + p) ^ (q << 2)) << 4);
  const int ra = rowoff + (lane & 15) * PITCH + g * 16;
#pragma unroll
  for (int s = 0; s < 4; ++s) {
    const u32x4 b = *(const u32x4 LAS*)(lds + ra + s * 64);
    u32x2 t00 = __builtin_bit_cast(u32x2, __builtin_amdgcn_ds_read_tr16_b64_v4i16((s16x4 LAS*)(lds + wa + s * 8192)));
    u32x2 t01 = __builtin_bit_cast(u32x2, __builtin_amdgcn_ds_read_tr16_b64_v4i16((s16x4 LAS*)(lds + wa + s * 8192 + 1024)));
    u32x2 t10 = __builtin_bit_cast(u32x2, __builtin_amdgcn_ds_read_tr16_b64_v4i16((s16x4 LAS*)(lds + wa + s * 8192 + 8)));
    u32x2 t11 = __builtin_bit_cast(u32x2, __builtin_amdgcn_ds_read_tr16_b64_v4i16((s16x4 LAS*)(lds + wa + s * 8192 + 1024 + 8)));
    o0 = mfma32((u32x4){t00.x, t00.y, t01.x, t01.y}, b, o0);
    o1 = mfma32((u32x4){t10.x, t10.y, t11.x, t11.y}, b, o1);
  }
}

struct Grp { u32x4 x0, x1, x2, x3; };

__global__ __launch_bounds__(1024) void main_kernel(const char* __restrict__ x, const int* __restrict__ src, const int* __restrict__ dst,
                                                    const char* __restrict__ wq, const char* __restrict__ wv,
                                                    const u32* __restrict__ slots, char* __restrict__ out, char* stamps) {
  __shared__ __attribute__((aligned(16))) char smem[L_END];
  char LAS* lds = (char LAS*)smem;
  const int tid = threadIdx.x, lane = tid & 63, b = blockIdx.x;
  const int w = __builtin_amdgcn_readfirstlane(tid >> 6);
  STAMP(4);
  const __amdgpu_buffer_rsrc_t xr = __builtin_amdgcn_make_buffer_rsrc((void*)x, 0, NN * 256, 0x00020000);

  const u32x4 sv = ((const u32x4*)slots)[b * (NC * 8) + tid];
  const int swz = ((lane & 15) ^ ((lane >> 4) << 2)) << 4;
  const int wsrc = (tid >> 4) * 256 + swz;
  const u32x4 xdv = __builtin_amdgcn_raw_buffer_load_b128(xr, ((tid >> 4) * NB + b) * 256 + (tid & 15) * 16, 0, 0);
  const u32x4 q0 = *(const u32x4*)(wq + wsrc), q1 = *(const u32x4*)(wq + wsrc + 16384);
  const u32x4 v0 = *(const u32x4*)(wv + wsrc), v1 = *(const u32x4*)(wv + wsrc + 16384);
  if (tid < NPB * CAP / 4) *(u32x4 LAS*)(lds + L_LIST + tid * 16) = (u32x4){EMPTY, EMPTY, EMPTY, EMPTY};
  if (tid < 64) *(u32 LAS*)(lds + L_CNT + tid * 4) = L_LIST + tid * CAP * 4;
  if (tid < 16) { *(u32 LAS*)(lds + L_Z + tid * 4) = 0; *(u32 LAS*)(lds + L_FLAG + tid * 4) = 0; }
  __syncthreads();
  STAMP(5);
  {
    u32 a[4];
#pragma unroll
    for (int j = 0; j < 4; ++j)
      if (sv[j] != EMPTY) a[j] = __hip_atomic_fetch_add((u32 LAS*)(lds + L_CNT + (sv[j] & 0xFC)), 4u, __ATOMIC_RELAXED, __HIP_MEMORY_SCOPE_WORKGROUP);
#pragma unroll
    for (int j = 0; j < 4; ++j)
      if (sv[j] != EMPTY) *(u32 LAS*)(lds + a[j]) = sv[j];
    if (sv.x & OVF) *(u32 LAS*)(lds + L_FLAG) = 1;
  }
  if (tid < 768) *(u32x4 LAS*)(lds + L_XD + (tid >> 4) * PITCH + (tid & 15) * 16) = xdv;
  *(u32x4 LAS*)(lds + L_WQ + tid * 16) = q0; *(u32x4 LAS*)(lds + L_WQ + 16384 + tid * 16) = q1;
  *(u32x4 LAS*)(lds + L_WV + tid * 16) = v0; *(u32x4 LAS*)(lds + L_WV + 16384 + tid * 16) = v1;
  __syncthreads();
  STAMP(6);

  const u32 cv = (*(const u32 LAS*)(lds + L_CNT + lane * 4) - (L_LIST + lane * CAP * 4)) >> 2;
  const bool slow = *(const u32 LAS*)(lds + L_FLAG) != 0 || __any(cv > CAP);
  const int g = lane >> 4, e = (lane >> 2) & 3, c = lane & 3;
  const int loff = (4 * g + c) * 16;
  const float kneg = -0.08838834764831845f * 1.4426950408889634f;

  int ln = w;
  int lu = slow ? 0 : (__builtin_amdgcn_readlane(cv, w) + 3) >> 2;
  int la = L_LIST + w * CAP * 4;
  Grp A, B;
  A.x0 = A.x1 = A.x2 = A.x3 = B.x0 = B.x1 = B.x2 = B.x3 = (u32x4){0, 0, 0, 0};
  u32 o0_, o1_, o2_, o3_;
  auto addrs = [&]() {
    const u32 LAS* lp = (const u32 LAS*)(lds + la + e * 4);
    const int adj = loff - ln * 4;
    o0_ = lp[0] + adj; o1_ = lp[4] + adj; o2_ = lp[8] + adj; o3_ = lp[12] + adj;
    asm volatile("" : "+v"(o0_), "+v"(o1_), "+v"(o2_), "+v"(o3_));
  };
  auto issue = [&](Grp& X) {
    if (lu > 0) X.x0 = __builtin_amdgcn_raw_buffer_load_b128(xr, o0_, 0, 0);
    if (lu > 1) X.x1 = __builtin_amdgcn_raw_buffer_load_b128(xr, o1_, 0, 0);
    if (lu > 2) X.x2 = __builtin_amdgcn_raw_buffer_load_b128(xr, o2_, 0, 0);
    if (lu > 3) X.x3 = __builtin_amdgcn_raw_buffer_load_b128(xr, o3_, 0, 0);
  };
  addrs();
  issue(A);

  if (w < 12) {
    f32x4 o0, o1;
    const int mt = w >> 2, cb = w & 3;
    proj_tile(lds, L_WQ, L_XD + mt * 16 * PITCH, cb, lane, o0, o1);
    *(u32x4 LAS*)(lds + L_QT + (mt * 16 + (lane & 15)) * PITCH + (4 * cb + g) * 16) =
        (u32x4){pk_bf16(o0.x, o0.y), pk_bf16(o0.z, o0.w), pk_bf16(o1.x, o1.y), pk_bf16(o1.z, o1.w)};
  }
  __builtin_amdgcn_s_waitcnt(0xC07F);
  __builtin_amdgcn_s_barrier();
  asm volatile("" ::: "memory");
  STAMP(7);

  if (!slow) {
    const u32 m0 = c == 0 ? 0x0000FFFFu : c == 1 ? 0xFFFF0000u : 0u;
    const u32 m1 = c == 2 ? 0x0000FFFFu : c == 3 ? 0xFFFF0000u : 0u;
    const int alw = L_AL + w * 512 + (g >> 1) * 256 + (lane & 15) * 16 + (g & 1) * 8;
    const int alr = L_AL + w * 512 + (lane & 15) * 16;
    u32x4 Q0, Q1, Q2, Q3;
    f32x4 acc0 = {0, 0, 0, 0}, acc1 = {0, 0, 0, 0};
    bool first = true;
    auto step = [&](Grp& X, Grp& Y) -> bool {
      const int cn = ln, cu = lu;
      if (first) {
        const int qa = L_QT + cn * PITCH + loff;
        Q0 = *(const u32x4 LAS*)(lds + (e == 0 ? qa : L_Z)); Q1 = *(const u32x4 LAS*)(lds + (e == 1 ? qa : L_Z));
        Q2 = *(const u32x4 LAS*)(lds + (e == 2 ? qa : L_Z)); Q3 = *(const u32x4 LAS*)(lds + (e == 3 ? qa : L_Z));
      }
      const bool last = cu <= 4;
      if (last) { ln += 16; lu = ln < NPB ? (__builtin_amdgcn_readlane(cv, ln) + 3) >> 2 : 0; la = L_LIST + ln * CAP * 4; }
      else { lu -= 4; la += 64; }
      addrs();
      asm volatile("" : "+v"(X.x0), "+v"(X.x1), "+v"(X.x2), "+v"(X.x3));
      issue(Y);
      f32x4 s = mfma32(Q0, X.x0, (f32x4){0, 0, 0, 0});
      s = mfma32(Q1, X.x1, s); s = mfma32(Q2, X.x2, s); s = mfma32(Q3, X.x3, s);
      const float s1 = s.x + dppa<0x55>(s.y), s2 = s.z + dppa<0xFF>(s.w);
      const float z = s1 + dppa<0xAA>(s2);
      const float al = __builtin_amdgcn_rcpf(1.0f + __builtin_amdgcn_exp2f(dppa<0x00>(z) * kneg));
      const u32 aa = pk_bf16(al, al);
      *(u32x2 LAS*)(lds + alw) = (u32x2){aa & m0, aa & m1};
      const u32x4 p0 = *(const u32x4 LAS*)(lds + alr), p1 = *(const u32x4 LAS*)(lds + alr + 256);
      if (cu > 0) { acc0 = mfma4(p0.x, p0.y, X.x0.x, X.x0.y, acc0); acc1 = mfma4(p0.x, p0.y, X.x0.z, X.x0.w, acc1); }
      if (cu > 1) { acc0 = mfma4(p0.z, p0.w, X.x1.x, X.x1.y, acc0); acc1 = mfma4(p0.z, p0.w, X.x1.z, X.x1.w, acc1); }
      if (cu > 2) { acc0 = mfma4(p1.x, p1.y, X.x2.x, X.x2.y, acc0); acc1 = mfma4(p1.x, p1.y, X.x2.z, X.x2.w, acc1); }
      if (cu > 3) { acc0 = mfma4(p1.z, p1.w, X.x3.x, X.x3.y, acc0); acc1 = mfma4(p1.z, p1.w, X.x3.z, X.x3.w, acc1); }
      first = last;
      if (last) {
        float m[4];
#pragma unroll
        for (int r = 0; r < 4; ++r) {
          const float t0 = acc0[r] + dppa<0x128>(acc0[r]), t1 = acc1[r] + dppa<0x128>(acc1[r]);
          m[r] = e < 2 ? t0 : t1;
        }
        const float a0 = m[0] + dppa<0x104>(m[0]), a1 = m[1] + dppa<0x104>(m[1]);
        const float a2 = m[2] + dppa<0x114>(m[2]), a3 = m[3] + dppa<0x114>(m[3]);
        const float u0 = (e & 1) ? a2 : a0, u1 = (e & 1) ? a3 : a1;
        *(u32 LAS*)(lds + L_AG + cn * PITCH + loff + e * 4) = pk_bf16(u0, u1);
        acc0 = (f32x4){0, 0, 0, 0}; acc1 = acc0;
        if (ln >= NPB) return false;
      }
      return true;
    };
    while (step(A, B) && step(B, A)) {}
  } else {
    float LAS* ag = (float LAS*)(lds + L_WQ);
    u32 LAS* qu = (u32 LAS*)(lds + L_LIST);
    u32 LAS* qn = (u32 LAS*)(lds + L_FLAG + 4);
    for (int i = tid; i < NPB * DD; i += 1024) ag[i] = 0.f;
    for (int base = 0; base < NE; base += 4096) {
      __syncthreads();
      if (tid == 0) *qn = 0;
      __syncthreads();
      const int i = base / 4 + tid;
      if (i < NE / 4) {
        const i32x4 d4 = ((const i32x4*)dst)[i];
#pragma unroll
        for (int j = 0; j < 4; ++j)
          if ((d4[j] & 255) == b) {
            const u32 p = __hip_atomic_fetch_add(qn, 1u, __ATOMIC_RELAXED, __HIP_MEMORY_SCOPE_WORKGROUP);
            qu[p] = ((u32)src[4 * i + j] << 8) | ((u32)d4[j] >> 8);
          }
      }
      __syncthreads();
      const int n = *qn;
      for (int k = w; k < n; k += 16) {
        const u32 rec = qu[k];
        const int nd = rec & 0xFF;
        const u32 xv = *(const u32*)(x + (rec & ~0xFFu) + lane * 4);
        const u32 qv = *(const u32 LAS*)(lds + L_QT + nd * PITCH + lane * 4);
        const float x0 = __builtin_bit_cast(float, xv << 16), x1 = __builtin_bit_cast(float, xv & 0xFFFF0000u);
        float t = x0 * __builtin_bit_cast(float, qv << 16) + x1 * __builtin_bit_cast(float, qv & 0xFFFF0000u);
#pragma unroll
        for (int o = 32; o > 0; o >>= 1) t += __shfl_xor(t, o);
        const float al0 = __builtin_amdgcn_rcpf(1.0f + __builtin_amdgcn_exp2f(t * kneg));
        const float al = __builtin_bit_cast(float, pk_bf16(al0, al0) << 16);
        __hip_atomic_fetch_add(ag + nd * DD + 2 * lane, al * x0, __ATOMIC_RELAXED, __HIP_MEMORY_SCOPE_WORKGROUP);
        __hip_atomic_fetch_add(ag + nd * DD + 2 * lane + 1, al * x1, __ATOMIC_RELAXED, __HIP_MEMORY_SCOPE_WORKGROUP);
      }
    }
    __syncthreads();
    for (int i = tid; i < NPB * 64; i += 1024)
      *(u32 LAS*)(lds + L_AG + (i >> 6) * PITCH + (i & 63) * 4) = pk_bf16(ag[2 * i], ag[2 * i + 1]);
  }
  STAMP(8);
  __syncthreads();
  STAMP(9);
  if (w < 12) {
    f32x4 o0, o1;
    const int mt = w >> 2, cb = w & 3;
    proj_tile(lds, L_WV, L_AG + mt * 16 * PITCH, cb, lane, o0, o1);
    const int node = (mt * 16 + (lane & 15)) * NB + b;
    if (node < NN)
      *(u32x4*)(out + (size_t)node * 256 + (4 * cb + g) * 16) =
          (u32x4){pk_bf16(o0.x, o0.y), pk_bf16(o0.z, o0.w), pk_bf16(o1.x, o1.y), pk_bf16(o1.z, o1.w)};
  }
  STAMP(10);
}

extern "C" void kernel_launch(void* const* d_in, const int* in_sizes, int n_in, void* d_out, int out_size, void* d_ws,
                              size_t ws_size, hipStream_t stream) {
  const char* x = (const char*)d_in[0];
  const int* src = (const int*)d_in[1];
  const int* dst = (const int*)d_in[2];
  const char* wq = (const char*)d_in[3];
  const char* wv = (const char*)d_in[4];
  u32* slots = (u32*)d_ws;
  char* stamps = (char*)d_ws + (8 << 20);
  bin_kernel<<<256, 1024, 0, stream>>>(src, dst, x, slots, stamps);
  main_kernel<<<NB, 1024, 0, stream>>>(x, src, dst, wq, wv, slots, (char*)d_out, stamps);
}
```

```cpp
#include <hip/hip_runtime.h>
#include <hip/hip_bf16.h>
#define NOPF

typedef unsigned u32;
typedef u32 u32x2 __attribute__((ext_vector_type(2)));
typedef u32 u32x4 __attribute__((ext_vector_type(4)));
typedef int i32x4 __attribute__((ext_vector_type(4)));
typedef float f32x4 __attribute__((ext_vector_type(4)));
typedef short s16x4 __attribute__((ext_vector_type(4)));
typedef __bf16 bf16x8 __attribute__((ext_vector_type(8)));
typedef __bf16 bf16x2 __attribute__((ext_vector_type(2)));
typedef float f32x2 __attribute__((ext_vector_type(2)));
#define LAS __attribute__((address_space(3)))

constexpr int NN = 10000, NE = 320000, DD = 128;
constexpr int NB = 256;
constexpr int NPB = 40;
constexpr int NC = 128;
constexpr int CH = NE / NC;
constexpr int SLOT = 32;
constexpr int CAP = 96;
constexpr u32 EMPTY = 0x003FFF00u;
constexpr u32 OVF = 1u;
static_assert(CH % 4 == 0 && CH * NC == NE, "chunking");

constexpr int PITCH = 272;
constexpr int L_WQ = 0, L_WV = 32768, L_XD = 65536, L_QT = L_XD + 48 * PITCH, L_AG = L_QT + 48 * PITCH,
              L_AL = L_AG + 48 * PITCH, L_Z = L_AL + 16 * 512, L_CNT = L_Z + 64, L_FLAG = L_CNT + 256,
              L_LIST = L_FLAG + 64, L_END = L_LIST + NPB * CAP * 4 + NC * SLOT * 4;

#define STAMP(i) do {} while (0)

__device__ __forceinline__ u32 pk_bf16(float lo, float hi) {
  f32x2 v = {lo, hi};
  bf16x2 b = __builtin_convertvector(v, bf16x2);
  return __builtin_bit_cast(u32, b);
}
__device__ __forceinline__ f32x4 mfma32(u32x4 a, u32x4 b, f32x4 c) {
  return __builtin_amdgcn_mfma_f32_16x16x32_bf16(__builtin_bit_cast(bf16x8, a), __builtin_bit_cast(bf16x8, b), c, 0, 0, 0);
}
__device__ __forceinline__ f32x4 mfma4(u32 a0, u32 a1, u32 b0, u32 b1, f32x4 c) {
  u32x2 a = {a0, a1}, b = {b0, b1};
  return __builtin_amdgcn_mfma_f32_4x4x4bf16_1k(__builtin_bit_cast(s16x4, a), __builtin_bit_cast(s16x4, b), c, 0, 0, 0);
}
template <int CTRL, int BANK> __device__ __forceinline__ float dpp(float old, float v) {
  return __builtin_bit_cast(float, __builtin_amdgcn_update_dpp(__builtin_bit_cast(int, old), __builtin_bit_cast(int, v), CTRL, 0xF, BANK, false));
}
template <int CTRL> __device__ __forceinline__ float dppa(float v) {
  return __builtin_bit_cast(float, __builtin_amdgcn_update_dpp(0, __builtin_bit_cast(int, v), CTRL, 0xF, 0xF, true));
}

__global__ __launch_bounds__(1024) void bin_kernel(const int* __restrict__ src, const int* __restrict__ dst,
                                                   const char* __restrict__ x, u32* __restrict__ slots, char* stamps) {
  __shared__ __attribute__((aligned(16))) u32 img[NB * SLOT];
  __shared__ u32 cnt[NB];
  const int tid = threadIdx.x, c = blockIdx.x;
  STAMP(0);
  if (c >= NC) {
    STAMP(3);
    return;
  }
  i32x4 d4 = {0, 0, 0, 0}, s4 = {0, 0, 0, 0};
  const bool act = tid < CH / 4;
  if (act) { d4 = ((const i32x4*)dst)[c * (CH / 4) + tid]; s4 = ((const i32x4*)src)[c * (CH / 4) + tid]; }
  const u32x4 e4 = {EMPTY, EMPTY, EMPTY, EMPTY};
  ((u32x4*)img)[tid] = e4; ((u32x4*)img)[tid + 1024] = e4;
  if (tid < NB) cnt[tid] = 0;
  __syncthreads();
  if (act) {
    u32 r[4];
#pragma unroll
    for (int j = 0; j < 4; ++j) r[j] = atomicAdd(&cnt[d4[j] & 255], 1u);
#pragma unroll
    for (int j = 0; j < 4; ++j) {
      const u32 b = d4[j] & 255;
      if (r[j] < SLOT) img[b * SLOT + ((r[j] + b) & (SLOT - 1))] = ((u32)s4[j] << 8) | (((u32)d4[j] >> 8) << 2);
    }
  }
  STAMP(1);
  __syncthreads();
#pragma unroll
  for (int k = 0; k < 2; ++k) {
    const int i = tid + k * 1024, b = i >> 3, p = i & 7;
    u32x4 v = ((const u32x4*)img)[i];
    if (cnt[b] > SLOT) v.x |= OVF;
    ((u32x4*)slots)[(b * NC + c) * 8 + p] = v;
  }
  STAMP(2);
}

__device__ __forceinline__ void proj_tile(char LAS* lds, int woff, int rowoff, int cb, int lane, f32x4& o0, f32x4& o1) {
  const int g = lane >> 4, q = (lane >> 2) & 3, p = lane & 3;
  o0 = (f32x4){0, 0, 0, 0}; o1 = o0;
  const int wa = woff + (8 * g + q) * 256 + (((4 * cb + p) ^ (q << 2)) << 4);
  const int ra = rowoff + (lane & 15) * PITCH + g * 16;
#pragma unroll
  for (int s = 0; s < 4; ++s) {
    const u32x4 b = *(const u32x4 LAS*)(lds + ra + s * 64);
    u32x2 t00 = __builtin_bit_cast(u32x2, __builtin_amdgcn_ds_read_tr16_b64_v4i16((s16x4 LAS*)(lds + wa + s * 8192)));
    u32x2 t01 = __builtin_bit_cast(u32x2, __builtin_amdgcn_ds_read_tr16_b64_v4i16((s16x4 LAS*)(lds + wa + s * 8192 + 1024)));
    u32x2 t10 = __builtin_bit_cast(u32x2, __builtin_amdgcn_ds_read_tr16_b64_v4i16((s16x4 LAS*)(lds + wa + s * 8192 + 8)));
    u32x2 t11 = __builtin_bit_cast(u32x2, __builtin_amdgcn_ds_read_tr16_b64_v4i16((s16x4 LAS*)(lds + wa + s * 8192 + 1024 + 8)));
    o0 = mfma32((u32x4){t00.x, t00.y, t01.x, t01.y}, b, o0);
    o1 = mfma32((u32x4){t10.x, t10.y, t11.x, t11.y}, b, o1);
  }
}

struct Grp { u32x4 x0, x1, x2, x3; };

__global__ __launch_bounds__(1024) void main_kernel(const char* __restrict__ x, const int* __restrict__ src, const int* __restrict__ dst,
                                                    const char* __restrict__ wq, const char* __restrict__ wv,
                                                    const u32* __restrict__ slots, char* __restrict__ out, char* stamps) {
  __shared__ __attribute__((aligned(16))) char smem[L_END];
  char LAS* lds = (char LAS*)smem;
  const int tid = threadIdx.x, lane = tid & 63, b = blockIdx.x;
  const int w = __builtin_amdgcn_readfirstlane(tid >> 6);
  STAMP(4);
  const __amdgpu_buffer_rsrc_t xr = __builtin_amdgcn_make_buffer_rsrc((void*)x, 0, NN * 256, 0x00020000);

  const u32x4 sv = ((const u32x4*)slots)[b * (NC * 8) + tid];
  const int swz = ((lane & 15) ^ ((lane >> 4) << 2)) << 4;
  const int wsrc = (tid >> 4) * 256 + swz;
  const u32x4 xdv = __builtin_amdgcn_raw_buffer_load_b128(xr, ((tid >> 4) * NB + b) * 256 + (tid & 15) * 16, 0, 0);
  const u32x4 q0 = *(const u32x4*)(wq + wsrc), q1 = *(const u32x4*)(wq + wsrc + 16384);
  const u32x4 v0 = *(const u32x4*)(wv + wsrc), v1 = *(const u32x4*)(wv + wsrc + 16384);
  if (tid < NPB * CAP / 4) *(u32x4 LAS*)(lds + L_LIST + tid * 16) = (u32x4){EMPTY, EMPTY, EMPTY, EMPTY};
  if (tid < 64) *(u32 LAS*)(lds + L_CNT + tid * 4) = L_LIST + tid * CAP * 4;
  if (tid < 16) { *(u32 LAS*)(lds + L_Z + tid * 4) = 0; *(u32 LAS*)(lds + L_FLAG + tid * 4) = 0; }
  __syncthreads();
  STAMP(5);
  {
    u32 a[4];
#pragma unroll
    for (int j = 0; j < 4; ++j)
      if (sv[j] != EMPTY) a[j] = __hip_atomic_fetch_add((u32 LAS*)(lds + L_CNT + (sv[j] & 0xFC)), 4u, __ATOMIC_RELAXED, __HIP_MEMORY_SCOPE_WORKGROUP);
#pragma unroll
    for (int j = 0; j < 4; ++j)
      if (sv[j] != EMPTY) *(u32 LAS*)(lds + a[j]) = sv[j];
    if (sv.x & OVF) *(u32 LAS*)(lds + L_FLAG) = 1;
  }
  if (tid < 768) *(u32x4 LAS*)(lds + L_XD + (tid >> 4) * PITCH + (tid & 15) * 16) = xdv;
  *(u32x4 LAS*)(lds + L_WQ + tid * 16) = q0; *(u32x4 LAS*)(lds + L_WQ + 16384 + tid * 16) = q1;
  *(u32x4 LAS*)(lds + L_WV + tid * 16) = v0; *(u32x4 LAS*)(lds + L_WV + 16384 + tid * 16) = v1;
  __syncthreads();
  STAMP(6);

  const u32 cv = (*(const u32 LAS*)(lds + L_CNT + lane * 4) - (L_LIST + lane * CAP * 4)) >> 2;
  const bool slow = *(const u32 LAS*)(lds + L_FLAG) != 0 || __any(cv > CAP);
  const int g = lane >> 4, e = (lane >> 2) & 3, c = lane & 3;
  const int loff = (4 * g + c) * 16;
  const float kneg = -0.08838834764831845f * 1.4426950408889634f;

  int ln = w;
  int lu = slow ? 0 : (__builtin_amdgcn_readlane(cv, w) + 3) >> 2;
  int la = L_LIST + w * CAP * 4;
  Grp A, B;
  A.x0 = A.x1 = A.x2 = A.x3 = B.x0 = B.x1 = B.x2 = B.x3 = (u32x4){0, 0, 0, 0};
  u32 o0_, o1_, o2_, o3_;
  auto addrs = [&]() {
    const u32 LAS* lp = (const u32 LAS*)(lds + la + e * 4);
    const int adj = loff - ln * 4;
    o0_ = lp[0] + adj; o1_ = lp[4] + adj; o2_ = lp[8] + adj; o3_ = lp[12] + adj;
    asm volatile("" : "+v"(o0_), "+v"(o1_), "+v"(o2_), "+v"(o3_));
  };
  auto issue = [&](Grp& X) {
    if (lu > 0) X.x0 = __builtin_amdgcn_raw_buffer_load_b128(xr, o0_, 0, 0);
    if (lu > 1) X.x1 = __builtin_amdgcn_raw_buffer_load_b128(xr, o1_, 0, 0);
    if (lu > 2) X.x2 = __builtin_amdgcn_raw_buffer_load_b128(xr, o2_, 0, 0);
    if (lu > 3) X.x3 = __builtin_amdgcn_raw_buffer_load_b128(xr, o3_, 0, 0);
  };
  addrs();
  issue(A);

  if (w < 12) {
    f32x4 o0, o1;
    const int mt = w >> 2, cb = w & 3;
    proj_tile(lds, L_WQ, L_XD + mt * 16 * PITCH, cb, lane, o0, o1);
    *(u32x4 LAS*)(lds + L_QT + (mt * 16 + (lane & 15)) * PITCH + (4 * cb + g) * 16) =
        (u32x4){pk_bf16(o0.x, o0.y), pk_bf16(o0.z, o0.w), pk_bf16(o1.x, o1.y), pk_bf16(o1.z, o1.w)};
  }
  __builtin_amdgcn_s_waitcnt(0xC07F);
  __builtin_amdgcn_s_barrier();
  asm volatile("" ::: "memory");
  STAMP(7);

  if (!slow) {
    const u32 m0 = c == 0 ? 0x0000FFFFu : c == 1 ? 0xFFFF0000u : 0u;
    const u32 m1 = c == 2 ? 0x0000FFFFu : c == 3 ? 0xFFFF0000u : 0u;
    const int alw = L_AL + w * 512 + (g >> 1) * 256 + (lane & 15) * 16 + (g & 1) * 8;
    const int alr = L_AL + w * 512 + (lane & 15) * 16;
    u32x4 Q0, Q1, Q2, Q3;
    f32x4 acc0 = {0, 0, 0, 0}, acc1 = {0, 0, 0, 0};
    bool first = true;
    auto step = [&](Grp& X, Grp& Y) -> bool {
      const int cn = ln, cu = lu;
      if (first) {
        const int qa = L_QT + cn * PITCH + loff;
        Q0 = *(const u32x4 LAS*)(lds + (e == 0 ? qa : L_Z)); Q1 = *(const u32x4 LAS*)(lds + (e == 1 ? qa : L_Z));
        Q2 = *(const u32x4 LAS*)(lds + (e == 2 ? qa : L_Z)); Q3 = *(const u32x4 LAS*)(lds + (e == 3 ? qa : L_Z));
      }
      const bool last = cu <= 4;
      if (last) { ln += 16; lu = ln < NPB ? (__builtin_amdgcn_readlane(cv, ln) + 3) >> 2 : 0; la = L_LIST + ln * CAP * 4; }
      else { lu -= 4; la += 64; }
      addrs();
      asm volatile("" : "+v"(X.x0), "+v"(X.x1), "+v"(X.x2), "+v"(X.x3));
      issue(Y);
      f32x4 s = mfma32(Q0, X.x0, (f32x4){0, 0, 0, 0});
      s = mfma32(Q1, X.x1, s); s = mfma32(Q2, X.x2, s); s = mfma32(Q3, X.x3, s);
      const float s1 = s.x + dppa<0x55>(s.y), s2 = s.z + dppa<0xFF>(s.w);
      const float z = s1 + dppa<0xAA>(s2);
      const float al = __builtin_amdgcn_rcpf(1.0f + __builtin_amdgcn_exp2f(dppa<0x00>(z) * kneg));
      const u32 aa = pk_bf16(al, al);
      *(u32x2 LAS*)(lds + alw) = (u32x2){aa & m0, aa & m1};
      const u32x4 p0 = *(const u32x4 LAS*)(lds + alr), p1 = *(const u32x4 LAS*)(lds + alr + 256);
      if (cu > 0) { acc0 = mfma4(p0.x, p0.y, X.x0.x, X.x0.y, acc0); acc1 = mfma4(p0.x, p0.y, X.x0.z, X.x0.w, acc1); }
      if (cu > 1) { acc0 = mfma4(p0.z, p0.w, X.x1.x, X.x1.y, acc0); acc1 = mfma4(p0.z, p0.w, X.x1.z, X.x1.w, acc1); }
      if (cu > 2) { acc0 = mfma4(p1.x, p1.y, X.x2.x, X.x2.y, acc0); acc1 = mfma4(p1.x, p1.y, X.x2.z, X.x2.w, acc1); }
      if (cu > 3) { acc0 = mfma4(p1.z, p1.w, X.x3.x, X.x3.y, acc0); acc1 = mfma4(p1.z, p1.w, X.x3.z, X.x3.w, acc1); }
      first = last;
      if (last) {
        float m[4];
#pragma unroll
        for (int r = 0; r < 4; ++r) {
          const float t0 = acc0[r] + dppa<0x128>(acc0[r]), t1 = acc1[r] + dppa<0x128>(acc1[r]);
          m[r] = e < 2 ? t0 : t1;
        }
        const float a0 = m[0] + dppa<0x104>(m[0]), a1 = m[1] + dppa<0x104>(m[1]);
        const float a2 = m[2] + dppa<0x114>(m[2]), a3 = m[3] + dppa<0x114>(m[3]);
        const float u0 = (e & 1) ? a2 : a0, u1 = (e & 1) ? a3 : a1;
        *(u32 LAS*)(lds + L_AG + cn * PITCH + loff + e * 4) = pk_bf16(u0, u1);
        acc0 = (f32x4){0, 0, 0, 0}; acc1 = acc0;
        if (ln >= NPB) return false;
      }
      return true;
    };
    while (step(A, B) && step(B, A)) {}
  } else {
    float LAS* ag = (float LAS*)(lds + L_WQ);
    u32 LAS* qu = (u32 LAS*)(lds + L_LIST);
    u32 LAS* qn = (u32 LAS*)(lds + L_FLAG + 4);
    for (int i = tid; i < NPB * DD; i += 1024) ag[i] = 0.f;
    for (int base = 0; base < NE; base += 4096) {
      __syncthreads();
      if (tid == 0) *qn = 0;
      __syncthreads();
      const int i = base / 4 + tid;
      if (i < NE / 4) {
        const i32x4 d4 = ((const i32x4*)dst)[i];
#pragma unroll
        for (int j = 0; j < 4; ++j)
          if ((d4[j] & 255) == b) {
            const u32 p = __hip_atomic_fetch_add(qn, 1u, __ATOMIC_RELAXED, __HIP_MEMORY_SCOPE_WORKGROUP);
            qu[p] = ((u32)src[4 * i + j] << 8) | ((u32)d4[j] >> 8);
          }
      }
      __syncthreads();
      const int n = *qn;
      for (int k = w; k < n; k += 16) {
        const u32 rec = qu[k];
        const int nd = rec & 0xFF;
        const u32 xv = *(const u32*)(x + (rec & ~0xFFu) + lane * 4);
        const u32 qv = *(const u32 LAS*)(lds + L_QT + nd * PITCH + lane * 4);
        const float x0 = __builtin_bit_cast(float, xv << 16), x1 = __builtin_bit_cast(float, xv & 0xFFFF0000u);
        float t = x0 * __builtin_bit_cast(float, qv << 16) + x1 * __builtin_bit_cast(float, qv & 0xFFFF0000u);
#pragma unroll
        for (int o = 32; o > 0; o >>= 1) t += __shfl_xor(t, o);
        const float al0 = __builtin_amdgcn_rcpf(1.0f + __builtin_amdgcn_exp2f(t * kneg));
        const float al = __builtin_bit_cast(float, pk_bf16(al0, al0) << 16);
        __hip_atomic_fetch_add(ag + nd * DD + 2 * lane, al * x0, __ATOMIC_RELAXED, __HIP_MEMORY_SCOPE_WORKGROUP);
        __hip_atomic_fetch_add(ag + nd * DD + 2 * lane + 1, al * x1, __ATOMIC_RELAXED, __HIP_MEMORY_SCOPE_WORKGROUP);
      }
    }
    __syncthreads();
    for (int i = tid; i < NPB * 64; i += 1024)
      *(u32 LAS*)(lds + L_AG + (i >> 6) * PITCH + (i & 63) * 4) = pk_bf16(ag[2 * i], ag[2 * i + 1]);
  }
  STAMP(8);
  __syncthreads();
  STAMP(9);
  if (w < 12) {
    f32x4 o0, o1;
    const int mt = w >> 2, cb = w & 3;
    proj_tile(lds, L_WV, L_AG + mt * 16 * PITCH, cb, lane, o0, o1);
    const int node = (mt * 16 + (lane & 15)) * NB + b;
    if (node < NN)
      *(u32x4*)(out + (size_t)node * 256 + (4 * cb + g) * 16) =
          (u32x4){pk_bf16(o0.x, o0.y), pk_bf16(o0.z, o0.w), pk_bf16(o1.x, o1.y), pk_bf16(o1.z, o1.w)};
  }
  STAMP(10);
}

extern "C" void kernel_launch(void* const* d_in, const int* in_sizes, int n_in, void* d_out, int out_size, void* d_ws,
                              size_t ws_size, hipStream_t stream) {
  const char* x = (const char*)d_in[0];
  const int* src = (const int*)d_in[1];
  const int* dst = (const int*)d_in[2];
  const char* wq = (const char*)d_in[3];
  const char* wv = (const char*)d_in[4];
  u32* slots = (u32*)d_ws;
  char* stamps = (char*)d_ws + (8 << 20);
  bin_kernel<<<256, 1024, 0, stream>>>(src, dst, x, slots, stamps);
  main_kernel<<<NB, 1024, 0, stream>>>(x, src, dst, wq, wv, slots, (char*)d_out, stamps);
}
```

```cpp
#include <hip/hip_runtime.h>
#include <hip/hip_bf16.h>

typedef unsigned u32;
typedef u32 u32x2 __attribute__((ext_vector_type(2)));
typedef u32 u32x4 __attribute__((ext_vector_type(4)));
typedef int i32x4 __attribute__((ext_vector_type(4)));
typedef float f32x4 __attribute__((ext_vector_type(4)));
typedef short s16x4 __attribute__((ext_vector_type(4)));
typedef __bf16 bf16x8 __attribute__((ext_vector_type(8)));
typedef __bf16 bf16x2 __attribute__((ext_vector_type(2)));
typedef float f32x2 __attribute__((ext_vector_type(2)));
#define LAS __attribute__((address_space(3)))

constexpr int NN = 10000, NE = 320000, DD = 128;
constexpr int NB = 256;
constexpr int NPB = 40;
constexpr int NC = 128;
constexpr int CH = NE / NC;
constexpr int SLOT = 32;
constexpr int CAP = 96;
constexpr u32 EMPTY = 0x003FFF00u;
#define PF 0
static_assert(CH % 4 == 0 && CH * NC == NE, "chunking");

constexpr int PITCH = 272;
constexpr int L_WQ = 0, L_WV = 32768, L_XD = 65536, L_QT = L_XD + 48 * PITCH, L_AG = L_QT + 48 * PITCH,
              L_AL = L_AG + 48 * PITCH, L_Z = L_AL + 16 * 512, L_CNT = L_Z + 64, L_FLAG = L_CNT + 256,
              L_LIST = L_FLAG + 64, L_END = L_LIST + NPB * CAP * 4 + NC * SLOT * 4;

#define STAMP(i) do {} while (0)
#define STAMP_DECL
#define STAMP_DUMP

__device__ __forceinline__ u32 pk_bf16(float lo, float hi) {
  f32x2 v = {lo, hi};
  bf16x2 b = __builtin_convertvector(v, bf16x2);
  return __builtin_bit_cast(u32, b);
}
__device__ __forceinline__ f32x4 mfma32(u32x4 a, u32x4 b, f32x4 c) {
  return __builtin_amdgcn_mfma_f32_16x16x32_bf16(__builtin_bit_cast(bf16x8, a), __builtin_bit_cast(bf16x8, b), c, 0, 0, 0);
}
__device__ __forceinline__ f32x4 mfma4(u32 a0, u32 a1, u32 b0, u32 b1, f32x4 c) {
  u32x2 a = {a0, a1}, b = {b0, b1};
  return __builtin_amdgcn_mfma_f32_4x4x4bf16_1k(__builtin_bit_cast(s16x4, a), __builtin_bit_cast(s16x4, b), c, 0, 0, 0);
}
template <int CTRL, int BANK> __device__ __forceinline__ float dpp(float old, float v) {
  return __builtin_bit_cast(float, __builtin_amdgcn_update_dpp(__builtin_bit_cast(int, old), __builtin_bit_cast(int, v), CTRL, 0xF, BANK, false));
}
template <int CTRL> __device__ __forceinline__ float dppa(float v) {
  return __builtin_bit_cast(float, __builtin_amdgcn_update_dpp(0, __builtin_bit_cast(int, v), CTRL, 0xF, 0xF, true));
}

__device__ __forceinline__ void proj_tile(char LAS* lds, int woff, int rowoff, int cb, int lane, f32x4& o0, f32x4& o1) {
  const int g = lane >> 4, q = (lane >> 2) & 3, p = lane & 3;
  o0 = (f32x4){0, 0, 0, 0}; o1 = o0;
  const int wa = woff + (8 * g + q) * 256 + (((4 * cb + p) ^ (q << 2)) << 4);
  const int ra = rowoff + (lane & 15) * PITCH + g * 16;
#pragma unroll
  for (int s = 0; s < 4; ++s) {
    const u32x4 b = *(const u32x4 LAS*)(lds + ra + s * 64);
    u32x2 t00 = __builtin_bit_cast(u32x2, __builtin_amdgcn_ds_read_tr16_b64_v4i16((s16x4 LAS*)(lds + wa + s * 8192)));
    u32x2 t01 = __builtin_bit_cast(u32x2, __builtin_amdgcn_ds_read_tr16_b64_v4i16((s16x4 LAS*)(lds + wa + s * 8192 + 1024)));
    u32x2 t10 = __builtin_bit_cast(u32x2, __builtin_amdgcn_ds_read_tr16_b64_v4i16((s16x4 LAS*)(lds + wa + s * 8192 + 8)));
    u32x2 t11 = __builtin_bit_cast(u32x2, __builtin_amdgcn_ds_read_tr16_b64_v4i16((s16x4 LAS*)(lds + wa + s * 8192 + 1024 + 8)));
    o0 = mfma32((u32x4){t00.x, t00.y, t01.x, t01.y}, b, o0);
    o1 = mfma32((u32x4){t10.x, t10.y, t11.x, t11.y}, b, o1);
  }
}

struct Grp { u32x4 x0, x1, x2, x3; };

constexpr u32 TAGM = 0xFFC00000u, TAG1 = 1u << 22;
constexpr int WS_EP = 0, WS_SLOT = 4096;
constexpr int L_IMG = L_QT, L_PCNT = L_IMG + NB * SLOT * 4;
static_assert(L_PCNT + NB * 4 <= L_Z, "producer image must stay below L_Z");
__global__ __launch_bounds__(1024, 4) void fused_kernel(const char* __restrict__ x, const int* __restrict__ src, const int* __restrict__ dst,
                                                    const char* __restrict__ wq, const char* __restrict__ wv,
                                                    char* ws, char* __restrict__ out) {
  __shared__ __attribute__((aligned(16))) char smem[L_END];
  char LAS* lds = (char LAS*)smem;
  const int tid = threadIdx.x, lane = tid & 63, b = blockIdx.x;
  const int w = __builtin_amdgcn_readfirstlane(tid >> 6);
  STAMP_DECL;
  STAMP(0);
  const __amdgpu_buffer_rsrc_t xr = __builtin_amdgcn_make_buffer_rsrc((void*)x, 0, NN * 256, 0x00020000);
  const __amdgpu_buffer_rsrc_t wr = __builtin_amdgcn_make_buffer_rsrc((void*)ws, 0, WS_SLOT + NB * NC * SLOT * 4, 0x00020000);
  const bool prod = b < NC;
  const bool act = prod && tid < CH / 4;
  i32x4 d4 = {0, 0, 0, 0}, s4 = {0, 0, 0, 0};
  u32x4 ep0 = {0, 0, 0, 0}, ep1 = {0, 0, 0, 0};
  if (act) { d4 = ((const i32x4*)dst)[b * (CH / 4) + tid]; s4 = ((const i32x4*)src)[b * (CH / 4) + tid]; }
  ep0 = __builtin_amdgcn_raw_buffer_load_b128(wr, WS_EP + (tid >> 3) * 16, 0, 0); ep1 = __builtin_amdgcn_raw_buffer_load_b128(wr, WS_EP + 2048 + (tid >> 3) * 16, 0, 0);
  __builtin_amdgcn_sched_barrier(0);
  u32x4 tag = *(const u32x4*)(ws + WS_EP + b * 16);
  const int swz = ((lane & 15) ^ ((lane >> 4) << 2)) << 4;
  const int wsrc = (tid >> 4) * 256 + swz;
  const u32x4 xdv = __builtin_amdgcn_raw_buffer_load_b128(xr, ((tid >> 4) * NB + b) * 256 + (tid & 15) * 16, 0, 0);
  const u32x4 q0 = *(const u32x4*)(wq + wsrc), q1 = *(const u32x4*)(wq + wsrc + 16384);
  const u32x4 v0 = *(const u32x4*)(wv + wsrc), v1 = *(const u32x4*)(wv + wsrc + 16384);
  const u32x4 e4 = {EMPTY, EMPTY, EMPTY, EMPTY};
  if (tid < NPB * CAP / 4) *(u32x4 LAS*)(lds + L_LIST + tid * 16) = e4;
  if (tid < 64) *(u32 LAS*)(lds + L_CNT + tid * 4) = L_LIST + tid * CAP * 4;
  if (tid < 16) { *(u32 LAS*)(lds + L_Z + tid * 4) = 0; *(u32 LAS*)(lds + L_FLAG + tid * 4) = 0; }
  if (prod) {
    *(u32x4 LAS*)(lds + L_IMG + tid * 16) = e4; *(u32x4 LAS*)(lds + L_IMG + 16384 + tid * 16) = e4;
    if (tid < NB) *(u32 LAS*)(lds + L_PCNT + tid * 4) = 0;
    __syncthreads();
    STAMP(1);
    if (act) {
      u32 r[4];
#pragma unroll
      for (int j = 0; j < 4; ++j) r[j] = __hip_atomic_fetch_add((u32 LAS*)(lds + L_PCNT + (d4[j] & 255) * 4), 4u, __ATOMIC_RELAXED, __HIP_MEMORY_SCOPE_WORKGROUP);
#pragma unroll
      for (int j = 0; j < 4; ++j) {
        const u32 bb = d4[j] & 255;
        const u32 rec = ((u32)s4[j] << 8) | (((u32)d4[j] >> 8) << 2);
        const u32 m = r[j] < 124u ? r[j] : 124u;
        *(u32 LAS*)(lds + L_IMG + (m ^ ((bb << 7) | ((bb & 31) << 2)))) = r[j] >= 124u ? (u32)(NPB << 2) : rec;
      }
    }
    STAMP(2);
    __syncthreads();
    const u32x4 t0 = (ep0 + TAG1) & TAGM, t1 = (ep1 + TAG1) & TAGM;
    const int so = WS_SLOT + ((tid >> 3) * NC + b) * 128 + (tid & 7) * 16;
    __builtin_amdgcn_raw_buffer_store_b128(*(const u32x4 LAS*)(lds + L_IMG + tid * 16) | t0, wr, so, 0, 16);
    __builtin_amdgcn_raw_buffer_store_b128(*(const u32x4 LAS*)(lds + L_IMG + 16384 + tid * 16) | t1, wr, so + 128 * NC * 128, 0, 16);
    STAMP(3);
  }
  tag = (tag + TAG1) & TAGM;
  if (tid < 768) *(u32x4 LAS*)(lds + L_XD + (tid >> 4) * PITCH + (tid & 15) * 16) = xdv;
  *(u32x4 LAS*)(lds + L_WQ + tid * 16) = q0; *(u32x4 LAS*)(lds + L_WQ + 16384 + tid * 16) = q1;
  *(u32x4 LAS*)(lds + L_WV + tid * 16) = v0; *(u32x4 LAS*)(lds + L_WV + 16384 + tid * 16) = v1;
  __syncthreads();
  STAMP(4);
  if (w < 12) {
    f32x4 o0, o1;
    const int mt = w >> 2, cb = w & 3;
    proj_tile(lds, L_WQ, L_XD + mt * 16 * PITCH, cb, lane, o0, o1);
    *(u32x4 LAS*)(lds + L_QT + (mt * 16 + (lane & 15)) * PITCH + (4 * cb + (lane >> 4)) * 16) =
        (u32x4){pk_bf16(o0.x, o0.y), pk_bf16(o0.z, o0.w), pk_bf16(o1.x, o1.y), pk_bf16(o1.z, o1.w)};
  }
  STAMP(5);
  {
    u32x4 sv;
    int spins = 0;
    for (;;) {
      asm volatile("buffer_load_dwordx4 %0, %1, %2, 0 offen sc1\n\ts_waitcnt vmcnt(0)" : "=&v"(sv) : "v"(WS_SLOT + b * (NC * 128) + tid * 16), "s"(wr) : "memory");
      sv ^= tag;
      if (__all((sv.x | sv.y | sv.z | sv.w) < TAG1)) break;
      if (++spins > (1 << 16)) { *(u32 LAS*)(lds + L_FLAG) = 1; sv = e4; break; }
      asm volatile("" ::: "memory");
    }
    STAMP(6);
    u32 a[4];
#pragma unroll
    for (int j = 0; j < 4; ++j)
      if (sv[j] != EMPTY) a[j] = __hip_atomic_fetch_add((u32 LAS*)(lds + L_CNT + (sv[j] & 0xFC)), 4u, __ATOMIC_RELAXED, __HIP_MEMORY_SCOPE_WORKGROUP);
#pragma unroll
    for (int j = 0; j < 4; ++j)
      if (sv[j] != EMPTY) *(u32 LAS*)(lds + a[j]) = sv[j];
  }
  __syncthreads();
  STAMP(7);
  if (tid == 0) __builtin_amdgcn_raw_buffer_store_b128(tag, wr, WS_EP + b * 16, 0, 16);

  const u32 cv = (*(const u32 LAS*)(lds + L_CNT + lane * 4) - (L_LIST + lane * CAP * 4)) >> 2;
  const bool slow = *(const u32 LAS*)(lds + L_FLAG) != 0 || __any(cv > (lane < NPB ? CAP : 0));
  const int g = lane >> 4, e = (lane >> 2) & 3, c = lane & 3;
  const int loff = (4 * g + c) * 16;
  const float kneg = -0.08838834764831845f * 1.4426950408889634f;

  int ln = w;
  int lu = slow ? 0 : (__builtin_amdgcn_readlane(cv, w) + 3) >> 2;
  int la = L_LIST + w * CAP * 4;
  Grp A, B;
  A.x0 = A.x1 = A.x2 = A.x3 = B.x0 = B.x1 = B.x2 = B.x3 = (u32x4){0, 0, 0, 0};
  u32 o0_, o1_, o2_, o3_;
  auto addrs = [&]() {
    const u32 LAS* lp = (const u32 LAS*)(lds + la + e * 4);
    const int adj = loff - ln * 4;
    o0_ = lp[0] + adj; o1_ = lp[4] + adj; o2_ = lp[8] + adj; o3_ = lp[12] + adj;
    asm volatile("" : "+v"(o0_), "+v"(o1_), "+v"(o2_), "+v"(o3_));
  };
  auto issue = [&](Grp& X) {
    if (lu > 0) X.x0 = __builtin_amdgcn_raw_buffer_load_b128(xr, o0_, 0, 0);
    if (lu > 1) X.x1 = __builtin_amdgcn_raw_buffer_load_b128(xr, o1_, 0, 0);
    if (lu > 2) X.x2 = __builtin_amdgcn_raw_buffer_load_b128(xr, o2_, 0, 0);
    if (lu > 3) X.x3 = __builtin_amdgcn_raw_buffer_load_b128(xr, o3_, 0, 0);
  };
  addrs();
  issue(A);

  if (!slow) {
    const u32 m0 = c == 0 ? 0x0000FFFFu : c == 1 ? 0xFFFF0000u : 0u;
    const u32 m1 = c == 2 ? 0x0000FFFFu : c == 3 ? 0xFFFF0000u : 0u;
    const int alw = L_AL + w * 512 + (g >> 1) * 256 + (lane & 15) * 16 + (g & 1) * 8;
    const int alr = L_AL + w * 512 + (lane & 15) * 16;
    u32x4 Q0, Q1, Q2, Q3;
    f32x4 acc0 = {0, 0, 0, 0}, acc1 = {0, 0, 0, 0};
    bool first = true;
    auto step = [&](Grp& X, Grp& Y) -> bool {
      const int cn = ln, cu = lu;
      if (first) {
        const int qa = L_QT + cn * PITCH + loff;
        Q0 = *(const u32x4 LAS*)(lds + (e == 0 ? qa : L_Z)); Q1 = *(const u32x4 LAS*)(lds + (e == 1 ? qa : L_Z));
        Q2 = *(const u32x4 LAS*)(lds + (e == 2 ? qa : L_Z)); Q3 = *(const u32x4 LAS*)(lds + (e == 3 ? qa : L_Z));
      }
      const bool last = cu <= 4;
      if (last) { ln += 16; lu = ln < NPB ? (__builtin_amdgcn_readlane(cv, ln) + 3) >> 2 : 0; la = L_LIST + ln * CAP * 4; }
      else { lu -= 4; la += 64; }
      addrs();
      asm volatile("" : "+v"(X.x0), "+v"(X.x1), "+v"(X.x2), "+v"(X.x3));
      issue(Y);
      f32x4 s = mfma32(Q0, X.x0, (f32x4){0, 0, 0, 0});
      s = mfma32(Q1, X.x1, s); s = mfma32(Q2, X.x2, s); s = mfma32(Q3, X.x3, s);
      const float s1 = s.x + dppa<0x55>(s.y), s2 = s.z + dppa<0xFF>(s.w);
      const float z = s1 + dppa<0xAA>(s2);
      const float al = __builtin_amdgcn_rcpf(1.0f + __builtin_amdgcn_exp2f(dppa<0x00>(z) * kneg));
      const u32 aa = pk_bf16(al, al);
      *(u32x2 LAS*)(lds + alw) = (u32x2){aa & m0, aa & m1};
      const u32x4 p0 = *(const u32x4 LAS*)(lds + alr), p1 = *(const u32x4 LAS*)(lds + alr + 256);
      if (cu > 0) { acc0 = mfma4(p0.x, p0.y, X.x0.x, X.x0.y, acc0); acc1 = mfma4(p0.x, p0.y, X.x0.z, X.x0.w, acc1); }
      if (cu > 1) { acc0 = mfma4(p0.z, p0.w, X.x1.x, X.x1.y, acc0); acc1 = mfma4(p0.z, p0.w, X.x1.z, X.x1.w, acc1); }
      if (cu > 2) { acc0 = mfma4(p1.x, p1.y, X.x2.x, X.x2.y, acc0); acc1 = mfma4(p1.x, p1.y, X.x2.z, X.x2.w, acc1); }
      if (cu > 3) { acc0 = mfma4(p1.z, p1.w, X.x3.x, X.x3.y, acc0); acc1 = mfma4(p1.z, p1.w, X.x3.z, X.x3.w, acc1); }
      first = last;
      if (last) {
        float m[4];
#pragma unroll
        for (int r = 0; r < 4; ++r) {
          const float t0 = acc0[r] + dppa<0x128>(acc0[r]), t1 = acc1[r] + dppa<0x128>(acc1[r]);
          m[r] = e < 2 ? t0 : t1;
        }
        const float a0 = m[0] + dppa<0x104>(m[0]), a1 = m[1] + dppa<0x104>(m[1]);
        const float a2 = m[2] + dppa<0x114>(m[2]), a3 = m[3] + dppa<0x114>(m[3]);
        const float u0 = (e & 1) ? a2 : a0, u1 = (e & 1) ? a3 : a1;
        *(u32 LAS*)(lds + L_AG + cn * PITCH + loff + e * 4) = pk_bf16(u0, u1);
        acc0 = (f32x4){0, 0, 0, 0}; acc1 = acc0;
        if (ln >= NPB) return false;
      }
      return true;
    };
    while (step(A, B) && step(B, A)) {}
  } else {
    float LAS* ag = (float LAS*)(lds + L_WQ);
    u32 LAS* qu = (u32 LAS*)(lds + L_LIST);
    u32 LAS* qn = (u32 LAS*)(lds + L_FLAG + 4);
    for (int i = tid; i < NPB * DD; i += 1024) ag[i] = 0.f;
    for (int base = 0; base < NE; base += 4096) {
      __syncthreads();
      if (tid == 0) *qn = 0;
      __syncthreads();
      const int i = base / 4 + tid;
      if (i < NE / 4) {
        const i32x4 d4 = ((const i32x4*)dst)[i];
#pragma unroll
        for (int j = 0; j < 4; ++j)
          if ((d4[j] & 255) == b) {
            const u32 p = __hip_atomic_fetch_add(qn, 1u, __ATOMIC_RELAXED, __HIP_MEMORY_SCOPE_WORKGROUP);
            qu[p] = ((u32)src[4 * i + j] << 8) | ((u32)d4[j] >> 8);
          }
      }
      __syncthreads();
      const int n = *qn;
      for (int k = w; k < n; k += 16) {
        const u32 rec = qu[k];
        const int nd = rec & 0xFF;
        const u32 xv = *(const u32*)(x + (rec & ~0xFFu) + lane * 4);
        const u32 qv = *(const u32 LAS*)(lds + L_QT + nd * PITCH + lane * 4);
        const float x0 = __builtin_bit_cast(float, xv << 16), x1 = __builtin_bit_cast(float, xv & 0xFFFF0000u);
        float t = x0 * __builtin_bit_cast(float, qv << 16) + x1 * __builtin_bit_cast(float, qv & 0xFFFF0000u);
#pragma unroll
        for (int o = 32; o > 0; o >>= 1) t += __shfl_xor(t, o);
        const float al0 = __builtin_amdgcn_rcpf(1.0f + __builtin_amdgcn_exp2f(t * kneg));
        const float al = __builtin_bit_cast(float, pk_bf16(al0, al0) << 16);
        __hip_atomic_fetch_add(ag + nd * DD + 2 * lane, al * x0, __ATOMIC_RELAXED, __HIP_MEMORY_SCOPE_WORKGROUP);
        __hip_atomic_fetch_add(ag + nd * DD + 2 * lane + 1, al * x1, __ATOMIC_RELAXED, __HIP_MEMORY_SCOPE_WORKGROUP);
      }
    }
    __syncthreads();
    for (int i = tid; i < NPB * 64; i += 1024)
      *(u32 LAS*)(lds + L_AG + (i >> 6) * PITCH + (i & 63) * 4) = pk_bf16(ag[2 * i], ag[2 * i + 1]);
  }
  STAMP(8);
  __syncthreads();
  STAMP(9);
  if (w < 12) {
    f32x4 o0, o1;
    const int mt = w >> 2, cb = w & 3;
    proj_tile(lds, L_WV, L_AG + mt * 16 * PITCH, cb, lane, o0, o1);
    const int node = (mt * 16 + (lane & 15)) * NB + b;
    if (node < NN)
      *(u32x4*)(out + (size_t)node * 256 + (4 * cb + g) * 16) =
          (u32x4){pk_bf16(o0.x, o0.y), pk_bf16(o0.z, o0.w), pk_bf16(o1.x, o1.y), pk_bf16(o1.z, o1.w)};
  }
  STAMP(10);
  STAMP_DUMP;
}

extern "C" void kernel_launch(void* const* d_in, const int* in_sizes, int n_in, void* d_out, int out_size, void* d_ws,
                              size_t ws_size, hipStream_t stream) {
  const char* x = (const char*)d_in[0];
  const int* src = (const int*)d_in[1];
  const int* dst = (const int*)d_in[2];
  const char* wq = (const char*)d_in[3];
  const char* wv = (const char*)d_in[4];
  fused_kernel<<<NB, 1024, 0, stream>>>(x, src, dst, wq, wv, (char*)d_ws, (char*)d_out);
}
```

```cpp
#include <hip/hip_runtime.h>
#include <hip/hip_bf16.h>

typedef unsigned u32;
typedef u32 u32x2 __attribute__((ext_vector_type(2)));
typedef u32 u32x4 __attribute__((ext_vector_type(4)));
typedef int i32x4 __attribute__((ext_vector_type(4)));
typedef float f32x4 __attribute__((ext_vector_type(4)));
typedef short s16x4 __attribute__((ext_vector_type(4)));
typedef __bf16 bf16x8 __attribute__((ext_vector_type(8)));
typedef __bf16 bf16x2 __attribute__((ext_vector_type(2)));
typedef float f32x2 __attribute__((ext_vector_type(2)));
#define LAS __attribute__((address_space(3)))

constexpr int NN = 10000, NE = 320000, DD = 128, NB = 256, NPB = 40;
constexpr int PITCH = 272, L_CNT = 0, L_WQ = 1280, L_QT = 47104, L_LIST = 60160, L_AG = 91904, L_WV = 104960, L_END = 141824;

__device__ __forceinline__ u32 pk_bf16(float lo, float hi) {
  f32x2 v = {lo, hi};
  return __builtin_bit_cast(u32, __builtin_convertvector(v, bf16x2));
}
__device__ __forceinline__ f32x4 mfma32(u32x2 a0, u32x2 a1, u32x4 b, f32x4 c) {
  const u32x4 a = {a0.x, a0.y, a1.x, a1.y};
  return __builtin_amdgcn_mfma_f32_16x16x32_bf16(__builtin_bit_cast(bf16x8, a), __builtin_bit_cast(bf16x8, b), c, 0, 0, 0);
}
__device__ __forceinline__ u32x2 tr_read(char LAS* p) {
  return __builtin_bit_cast(u32x2, __builtin_amdgcn_ds_read_tr16_b64_v4i16((s16x4 LAS*)p));
}

__global__ __launch_bounds__(1024, 4) void featgraph_kernel(const char* x, const int* src, const int* dst, const char* wq,
                                                            const char* wv, char* ws, char* out) {
  __shared__ __attribute__((aligned(16))) char smem[L_END];
  asm volatile(
      ".Lfg_begin:\n"
      "s_load_dwordx8 s[4:11], s[0:1], 0\n"
      "s_load_dwordx4 s[12:15], s[0:1], 32\n"
      "s_load_dwordx2 s[16:17], s[0:1], 48\n"
      "s_memrealtime s[18:19]\n"
      "v_readfirstlane_b32 s3, v0\n"
      "v_and_b32 v1, 63, v0\n"
      "v_lshlrev_b32 v2, 4, v0\n"
      "s_lshr_b32 s3, s3, 6\n"
      "s_cmp_lg_u32 s3, 15\n"
      "s_cbranch_scc1 .Lfg_nocpf\n"
      "s_getpc_b64 s[36:37]\n"
      ".Lfg_pc0:\n"
      "v_lshlrev_b32 v3, 7, v1\n"
      "v_min_u32 v3, .Lfg_end-.Lfg_pc0-4, v3\n"
      "global_load_dword v7, v3, s[36:37]\n"
      ".Lfg_nocpf:\n"
      "s_mul_i32 s48, s2, 10000\n"
      "v_lshrrev_b32 v4, 3, v0\n"
      "v_add_u32 v3, s48, v2\n"
      "v_cmp_gt_u32 vcc, 625, v0\n"
      "v_lshlrev_b32 v5, 2, v4\n"
      "s_lshl_b32 s33, s2, 2\n"
      "s_mov_b64 s[34:35], vcc\n"
      "s_cmp_ge_u32 s2, 128\n"
      "s_waitcnt lgkmcnt(0)\n"
      "s_cbranch_scc1 .Lfg_noidx\n"
      "s_mov_b64 exec, s[34:35]\n"
      "s_cbranch_execz .Lfg_noidx2\n"
      "global_load_dwordx4 v[8:11], v3, s[8:9]\n"
      "global_load_dwordx4 v[12:15], v3, s[6:7]\n"
      ".Lfg_noidx2:\n"
      "s_mov_b64 exec, -1\n"
      "global_load_dword v16, v5, s[14:15]\n"
      "global_load_dword v17, v5, s[14:15] offset:512\n"
      ".Lfg_noidx:\n"
      "s_mov_b32 s20, s4\n"
      "s_and_b32 s21, s5, 0xffff\n"
      "s_mov_b32 s22, 2560000\n"
      "s_mov_b32 s23, 0x20000\n"
      "s_mov_b32 s24, s14\n"
      "s_and_b32 s25, s15, 0xffff\n"
      "s_mov_b32 s26, 4198400\n"
      "s_mov_b32 s27, 0x20000\n"
      "s_mov_b32 s28, s16\n"
      "s_and_b32 s29, s17, 0xffff\n"
      "s_mov_b32 s30, 2560000\n"
      "s_mov_b32 s31, 0x20000\n"
      "v_mov_b32 v28, 4194303\n"
      "v_mov_b32 v29, v28\n"
      "v_mov_b32 v30, v28\n"
      "v_mov_b32 v31, v28\n"
      "v_lshl_add_u32 v6, v4, 4, v2\n"
      "v_add_u32 v6, 104960, v6\n"
      "ds_write_b128 v2, v[28:31] offset:60160\n"
      "ds_write_b128 v6, v[28:31]\n"
      "ds_write_b128 v6, v[28:31] offset:18432\n"
      "s_cmp_gt_u32 s3, 4\n"
      "s_cbranch_scc1 .Lfg_nocnt\n"
      "s_movk_i32 s36, 144\n"
      "s_mov_b32 s37, 104960\n"
      "s_movk_i32 s38, 256\n"
      "s_cmp_eq_u32 s3, 4\n"
      "s_cmovk_i32 s36, 384\n"
      "s_cmov_b32 s37, -38144\n"
      "s_cmovk_i32 s38, -1024\n"
      "v_mov_b32 v18, s37\n"
      "v_lshl_add_u32 v19, v0, 2, s38\n"
      "v_mad_u32_u24 v18, v0, s36, v18\n"
      "ds_write_b32 v19, v18\n"
      ".Lfg_nocnt:\n"
      "v_lshrrev_b32 v44, 4, v1\n"
      "v_and_b32 v45, 3, v1\n"
      "v_bfe_u32 v46, v1, 2, 2\n"
      "v_and_b32 v47, 15, v1\n"
      "v_mov_b64 v[48:49], 0\n"
      "v_mov_b64 v[50:51], 0\n"
      "v_mov_b64 v[52:53], 0\n"
      "v_mov_b64 v[54:55], 0\n"
      "v_mov_b64 v[56:57], 0\n"
      "v_mov_b64 v[58:59], 0\n"
      "v_mov_b64 v[60:61], 0\n"
      "v_mov_b64 v[62:63], 0\n"
      "v_mov_b64 v[64:65], 0\n"
      "v_mov_b64 v[66:67], 0\n"
      "v_mov_b64 v[68:69], 0\n"
      "v_mov_b64 v[70:71], 0\n"
      "v_mov_b64 v[72:73], 0\n"
      "v_mov_b64 v[74:75], 0\n"
      "v_mov_b64 v[76:77], 0\n"
      "v_mov_b64 v[78:79], 0\n"
      "v_mov_b64 v[80:81], 0\n"
      "v_mov_b64 v[82:83], 0\n"
      "v_mov_b64 v[84:85], 0\n"
      "v_mov_b64 v[86:87], 0\n"
      "v_mov_b64 v[88:89], 0\n"
      "v_mov_b64 v[90:91], 0\n"
      "v_mov_b64 v[92:93], 0\n"
      "v_mov_b64 v[94:95], 0\n"
      "v_mov_b64 v[96:97], 0\n"
      "v_mov_b64 v[98:99], 0\n"
      "v_mov_b64 v[100:101], 0\n"
      "v_mov_b64 v[102:103], 0\n"
      "v_mov_b64 v[104:105], 0\n"
      "v_mov_b64 v[106:107], 0\n"
      "v_mov_b64 v[108:109], 0\n"
      "v_mov_b64 v[110:111], 0\n"
      "s_waitcnt lgkmcnt(0)\n"
      "s_barrier\n"
      "s_cmp_lt_u32 s3, 10\n"
      "s_cbranch_scc1 .Lfg_role_lo\n"
      "s_cmp_ge_u32 s3, 14\n"
      "s_cbranch_scc1 .Lfg_xd\n"
      "v_lshlrev_b32 v18, 2, v44\n"
      "v_xor_b32 v18, v18, v47\n"
      "v_lshlrev_b32 v18, 4, v18\n"
      "v_lshl_add_u32 v18, v44, 8, v18\n"
      "s_lshr_b32 s37, s2, 3\n"
      "s_add_u32 s37, s37, s3\n"
      "s_and_b32 s37, s37, 3\n"
      "s_lshl_b32 s36, s37, 13\n"
      "s_add_u32 m0, s36, 1280\n"
      "v_add_u32 v18, s36, v18\n"
      "global_load_lds_dwordx4 v18, s[10:11] offset:0\n"
      "global_load_lds_dwordx4 v18, s[10:11] offset:1024\n"
      "global_load_lds_dwordx4 v18, s[10:11] offset:2048\n"
      "global_load_lds_dwordx4 v18, s[10:11] offset:3072\n"
      "s_add_u32 m0, m0, 4096\n"
      "v_add_u32 v18, 4096, v18\n"
      "global_load_lds_dwordx4 v18, s[10:11] offset:0\n"
      "global_load_lds_dwordx4 v18, s[10:11] offset:1024\n"
      "global_load_lds_dwordx4 v18, s[10:11] offset:2048\n"
      "global_load_lds_dwordx4 v18, s[10:11] offset:3072\n"
      "s_waitcnt vmcnt(8)\n"
      "s_branch .Lfg_role_end\n"
      ".Lfg_xd:\n"
      "v_xor_b32 v18, v44, v47\n"
      "v_lshlrev_b32 v18, 4, v18\n"
      "v_lshl_add_u32 v18, v44, 16, v18\n"
      "s_sub_u32 s37, s3, 14\n"
      "s_mul_i32 s36, s37, 1310720\n"
      "s_lshl_b32 s38, s2, 8\n"
      "s_add_u32 s36, s36, s38\n"
      "s_mul_i32 s37, s37, 5440\n"
      "s_add_u32 m0, s37, 34048\n"
      "v_add_u32 v18, s36, v18\n"
      "buffer_load_dwordx4 v18, s[20:23], 0 offen lds\n"
      "s_add_u32 m0, m0, 1088\n"
      "v_add_u32 v18, 262144, v18\n"
      "buffer_load_dwordx4 v18, s[20:23], 0 offen lds\n"
      "s_add_u32 m0, m0, 1088\n"
      "v_add_u32 v18, 262144, v18\n"
      "buffer_load_dwordx4 v18, s[20:23], 0 offen lds\n"
      "s_add_u32 m0, m0, 1088\n"
      "v_add_u32 v18, 262144, v18\n"
      "buffer_load_dwordx4 v18, s[20:23], 0 offen lds\n"
      "s_add_u32 m0, m0, 1088\n"
      "v_add_u32 v18, 262144, v18\n"
      "buffer_load_dwordx4 v18, s[20:23], 0 offen lds\n"
      "s_waitcnt vmcnt(5)\n"
      "s_branch .Lfg_role_end\n"
      ".Lfg_role_lo:\n"
      "s_cmp_ge_u32 s2, 128\n"
      "s_cbranch_scc1 .Lfg_pf\n"
      "s_mov_b64 exec, s[34:35]\n"
      "v_mov_b32 v26, 4\n"
      "v_mov_b32 v27, 105084\n"
      "s_mov_b32 s36, 0x0c050401\n"
      "s_waitcnt vmcnt(3)\n"
      "v_lshlrev_b32_sdwa v22, 2, v8 dst_sel:DWORD dst_unused:UNUSED_PAD src0_sel:DWORD src1_sel:BYTE_0\n"
      "ds_add_rtn_u32 v18, v22, v26 offset:256\n"
      "v_lshlrev_b32_sdwa v23, 2, v9 dst_sel:DWORD dst_unused:UNUSED_PAD src0_sel:DWORD src1_sel:BYTE_0\n"
      "ds_add_rtn_u32 v19, v23, v26 offset:256\n"
      "v_lshlrev_b32_sdwa v24, 2, v10 dst_sel:DWORD dst_unused:UNUSED_PAD src0_sel:DWORD src1_sel:BYTE_0\n"
      "ds_add_rtn_u32 v20, v24, v26 offset:256\n"
      "v_lshlrev_b32_sdwa v25, 2, v11 dst_sel:DWORD dst_unused:UNUSED_PAD src0_sel:DWORD src1_sel:BYTE_0\n"
      "ds_add_rtn_u32 v21, v25, v26 offset:256\n"
      "v_mad_u32_u24 v22, v22, 36, v27\n"
      "v_mad_u32_u24 v23, v23, 36, v27\n"
      "v_mad_u32_u24 v24, v24, 36, v27\n"
      "v_mad_u32_u24 v25, v25, 36, v27\n"
      "s_waitcnt vmcnt(2)\n"
      "v_perm_b32 v12, v12, v8, s36\n"
      "v_perm_b32 v13, v13, v9, s36\n"
      "v_perm_b32 v14, v14, v10, s36\n"
      "v_perm_b32 v15, v15, v11, s36\n"
      "s_waitcnt lgkmcnt(3)\n"
      "v_min_u32 v18, v18, v22\n"
      "ds_write_b32 v18, v12\n"
      "s_waitcnt lgkmcnt(3)\n"
      "v_min_u32 v19, v19, v23\n"
      "ds_write_b32 v19, v13\n"
      "s_waitcnt lgkmcnt(3)\n"
      "v_min_u32 v20, v20, v24\n"
      "ds_write_b32 v20, v14\n"
      "s_waitcnt lgkmcnt(3)\n"
      "v_min_u32 v21, v21, v25\n"
      "ds_write_b32 v21, v15\n"
      "s_mov_b64 exec, -1\n"
      "s_waitcnt vmcnt(0)\n"
      ".Lfg_role_end:\n"
      "s_cmp_ge_u32 s2, 128\n"
      "s_cbranch_scc1 .Lfg_np\n"
      "v_add_u32 v16, 0x400000, v16\n"
      "v_add_u32 v17, 0x400000, v17\n"
      "v_and_b32 v16, 0xffc00000, v16\n"
      "v_and_b32 v17, 0xffc00000, v17\n"
      "v_and_b32 v18, 0x70, v2\n"
      "v_lshl_add_u32 v18, v4, 14, v18\n"
      "s_lshl_b32 s36, s2, 7\n"
      "s_add_u32 s36, s36, 4096\n"
      "s_add_u32 s37, s36, 2097152\n"
      "s_waitcnt lgkmcnt(0)\n"
      "s_barrier\n"
      "ds_read_b128 v[8:11], v6\n"
      "ds_read_b128 v[12:15], v6 offset:18432\n"
      "s_waitcnt lgkmcnt(1)\n"
      "v_or_b32 v8, v8, v16\n"
      "v_or_b32 v9, v9, v16\n"
      "v_or_b32 v10, v10, v16\n"
      "v_or_b32 v11, v11, v16\n"
      "buffer_store_dwordx4 v[8:11], v18, s[24:27], s36 offen sc1\n"
      "s_waitcnt lgkmcnt(0)\n"
      "v_or_b32 v12, v12, v17\n"
      "v_or_b32 v13, v13, v17\n"
      "v_or_b32 v14, v14, v17\n"
      "v_or_b32 v15, v15, v17\n"
      "buffer_store_dwordx4 v[12:15], v18, s[24:27], s37 offen sc1\n"
      "s_waitcnt vmcnt(2)\n"
      "s_branch .Lfg_join\n"
      ".Lfg_pf:\n"
      "s_cmp_lg_u32 s3, 0\n"
      "s_cbranch_scc1 .Lfg_pf_w\n"
      "s_lshr_b32 s36, s2, 3\n"
      "s_and_b32 s36, s36, 3\n"
      "s_lshl_b32 s36, s36, 13\n"
      "v_lshl_add_u32 v18, v1, 7, s36\n"
      "global_load_dword v7, v18, s[12:13]\n"
      ".Lfg_pf_w:\n"
      "s_branch .Lfg_join\n"
      ".Lfg_np:\n"
      "s_waitcnt vmcnt(0)\n"
      ".Lfg_join:\n"
      "s_load_dword s33, s[14:15], s33\n"
      "s_barrier\n"
      "v_and_b32 v18, 7, v0\n"
      "v_lshlrev_b32 v18, 3, v18\n"
      "v_lshl_add_u32 v18, v4, 7, v18\n"
      "s_lshl_b32 s39, s2, 14\n"
      "s_add_u32 s39, s39, 4096\n"
      "v_and_b32 v19, 1, v44\n"
      "v_lshlrev_b32 v19, 3, v19\n"
      "s_and_b32 s36, s3, 1\n"
      "s_lshl_b32 s36, s36, 2\n"
      "v_xor_b32 v19, v19, v47\n"
      "v_xor_b32 v19, s36, v19\n"
      "v_lshlrev_b32 v19, 4, v19\n"
      "v_lshl_add_u32 v19, v44, 8, v19\n"
      "s_lshl_b32 s36, s3, 10\n"
      "v_add_u32 v19, s36, v19\n"
      "v_add_u32 v20, 0x4000, v19\n"
      "s_add_u32 m0, s36, 104960\n"
      "s_waitcnt lgkmcnt(0)\n"
      "s_add_u32 s33, s33, 0x400000\n"
      "s_and_b32 s33, s33, 0xffc00000\n"
      "s_add_u32 s18, s18, 190\n"
      ".Lfg_dl:\n"
      "s_memrealtime s[36:37]\n"
      "s_waitcnt lgkmcnt(0)\n"
      "s_sub_u32 s36, s36, s18\n"
      "s_cmp_lt_i32 s36, 0\n"
      "s_cbranch_scc1 .Lfg_dl\n"
      "buffer_load_dwordx2 v[2:3], v18, s[24:27], s39 offen sc1\n"
      "buffer_load_dwordx2 v[4:5], v18, s[24:27], s39 offen offset:64 sc1\n"
      "global_load_lds_dwordx4 v19, s[12:13]\n"
      "s_add_u32 m0, m0, 0x4000\n"
      "s_nop 0\n"
      "global_load_lds_dwordx4 v20, s[12:13]\n"
      "s_and_b32 s40, s3, 3\n"
      "s_lshr_b32 s41, s3, 2\n"
      "v_xor_b32 v20, s40, v46\n"
      "v_lshl_add_u32 v20, v20, 2, v45\n"
      "v_lshlrev_b32 v20, 4, v20\n"
      "v_lshl_add_u32 v20, v44, 11, v20\n"
      "s_cmp_ge_u32 s3, 12\n"
      "s_cbranch_scc1 .Lfg_noq\n"
      "v_lshl_add_u32 v21, v46, 8, v20\n"
      "v_xor_b32 v22, v44, v45\n"
      "v_lshlrev_b32 v22, 4, v22\n"
      "v_lshl_add_u32 v22, v45, 8, v22\n"
      "s_movk_i32 s36, 1088\n"
      "v_mad_u32_u24 v22, v46, s36, v22\n"
      "s_mul_i32 s37, s41, 4352\n"
      "v_add_u32 v22, s37, v22\n"
      "ds_read_b128 v[112:115], v22 offset:34048\n"
      "ds_read_b64_tr_b16 v[64:65], v21 offset:1280\n"
      "ds_read_b64_tr_b16 v[66:67], v21 offset:2304\n"
      "ds_read_b64_tr_b16 v[68:69], v21 offset:1288\n"
      "ds_read_b64_tr_b16 v[70:71], v21 offset:2312\n"
      "ds_read_b128 v[116:119], v22 offset:34112\n"
      "ds_read_b64_tr_b16 v[72:73], v21 offset:9472\n"
      "ds_read_b64_tr_b16 v[74:75], v21 offset:10496\n"
      "ds_read_b64_tr_b16 v[76:77], v21 offset:9480\n"
      "ds_read_b64_tr_b16 v[78:79], v21 offset:10504\n"
      "ds_read_b128 v[120:123], v22 offset:34176\n"
      "ds_read_b64_tr_b16 v[80:81], v21 offset:17664\n"
      "ds_read_b64_tr_b16 v[82:83], v21 offset:18688\n"
      "ds_read_b64_tr_b16 v[84:85], v21 offset:17672\n"
      "ds_read_b64_tr_b16 v[86:87], v21 offset:18696\n"
      "s_waitcnt lgkmcnt(10)\n"
      "v_mfma_f32_16x16x32_bf16 v[8:11], v[64:67], v[112:115], 0\n"
      "v_mfma_f32_16x16x32_bf16 v[12:15], v[68:71], v[112:115], 0\n"
      "ds_read_b128 v[124:127], v22 offset:34240\n"
      "ds_read_b64_tr_b16 v[88:89], v21 offset:25856\n"
      "ds_read_b64_tr_b16 v[90:91], v21 offset:26880\n"
      "ds_read_b64_tr_b16 v[92:93], v21 offset:25864\n"
      "ds_read_b64_tr_b16 v[94:95], v21 offset:26888\n"
      "s_mul_i32 s37, s41, 4352\n"
      "s_lshl_b32 s38, s40, 6\n"
      "s_add_u32 s37, s37, s38\n"
      "s_movk_i32 s36, 272\n"
      "v_lshl_add_u32 v22, v44, 4, s37\n"
      "v_mad_u32_u24 v22, v47, s36, v22\n"
      "s_waitcnt lgkmcnt(10)\n"
      "v_mfma_f32_16x16x32_bf16 v[8:11], v[72:75], v[116:119], v[8:11]\n"
      "v_mfma_f32_16x16x32_bf16 v[12:15], v[76:79], v[116:119], v[12:15]\n"
      "s_waitcnt lgkmcnt(5)\n"
      "v_mfma_f32_16x16x32_bf16 v[8:11], v[80:83], v[120:123], v[8:11]\n"
      "v_mfma_f32_16x16x32_bf16 v[12:15], v[84:87], v[120:123], v[12:15]\n"
      "s_waitcnt lgkmcnt(0)\n"
      "v_mfma_f32_16x16x32_bf16 v[8:11], v[88:91], v[124:127], v[8:11]\n"
      "v_mfma_f32_16x16x32_bf16 v[12:15], v[92:95], v[124:127], v[12:15]\n"
      "s_nop 6\n"
      "v_cvt_pk_bf16_f32 v8, v8, v9\n"
      "v_cvt_pk_bf16_f32 v9, v10, v11\n"
      "v_cvt_pk_bf16_f32 v10, v12, v13\n"
      "v_cvt_pk_bf16_f32 v11, v14, v15\n"
      "ds_write_b128 v22, v[8:11] offset:47104\n"
      ".Lfg_noq:\n"
      "v_mov_b32 v16, v20\n"
      "v_mov_b32 v17, v44\n"
      "v_lshl_add_u32 v22, v44, 2, v45\n"
      "v_lshlrev_b32 v22, 4, v22\n"
      "v_lshl_add_u32 v20, v45, 2, v46\n"
      "s_mul_i32 s36, s3, 384\n"
      "v_lshl_add_u32 v20, v20, 2, s36\n"
      "s_mul_i32 s36, s3, 272\n"
      "v_add_u32 v19, s36, v22\n"
      "s_add_u32 s36, s36, 91904\n"
      "v_lshl_add_u32 v6, v1, 2, s36\n"
      "v_subrev_u32 v29, s3, v22\n"
      "v_and_b32 v24, 1, v45\n"
      "v_lshlrev_b32 v24, 4, v24\n"
      "v_mov_b32 v25, 0xffff\n"
      "v_lshlrev_b32 v25, v24, v25\n"
      "v_cmp_gt_u32 vcc, 2, v45\n"
      "v_mov_b32 v23, 0xbe0293ee\n"
      "v_mov_b64 v[40:41], 0\n"
      "v_mov_b64 v[42:43], 0\n"
      "v_cndmask_b32 v24, 0, v25, vcc\n"
      "v_xor_b32 v25, v25, v24\n"
      "v_mov_b64 v[32:33], 0\n"
      "v_mov_b64 v[34:35], 0\n"
      "v_mov_b64 v[36:37], 0\n"
      "v_mov_b64 v[38:39], 0\n"
      "v_lshlrev_b32 v15, 2, v1\n"
      "s_movk_i32 s36, 384\n"
      "v_mov_b32 v14, 60148\n"
      "v_mad_u32_u24 v14, v1, s36, v14\n"
      "v_mov_b32 v13, 4\n"
      "s_mov_b32 s38, 4194303\n"
      "s_mov_b32 s50, 0x000f000f\n"
      "s_mov_b32 s51, s50\n"
      "s_lshl_b64 s[52:53], s[50:51], 4\n"
      "s_lshl_b64 s[54:55], s[50:51], 8\n"
      "s_lshl_b64 s[56:57], s[50:51], 12\n"
      "s_getpc_b64 s[58:59]\n"
      ".Lfg_pc1:\n"
      "s_add_u32 s58, s58, .Lfg_ldend-.Lfg_pc1\n"
      "s_addc_u32 s59, s59, 0\n"
      "s_mov_b32 s42, 0\n"
      "s_waitcnt vmcnt(2)\n"
      ".Lfg_chk:\n"
      "v_xor_b32 v2, s33, v2\n"
      "v_xor_b32 v3, s33, v3\n"
      "v_xor_b32 v4, s33, v4\n"
      "v_xor_b32 v5, s33, v5\n"
      "v_or3_b32 v8, v2, v3, v4\n"
      "v_cmp_ne_u32 s[44:45], s38, v2\n"
      "v_or_b32 v8, v8, v5\n"
      "v_cmp_ne_u32 s[46:47], s38, v3\n"
      "v_cmp_lt_u32 vcc, s38, v8\n"
      "v_lshlrev_b32_sdwa v9, 2, v2 dst_sel:DWORD dst_unused:UNUSED_PAD src0_sel:DWORD src1_sel:BYTE_0\n"
      "v_lshlrev_b32_sdwa v10, 2, v3 dst_sel:DWORD dst_unused:UNUSED_PAD src0_sel:DWORD src1_sel:BYTE_0\n"
      "s_cbranch_vccnz .Lfg_retry\n"
      "v_cmp_ne_u32 vcc, s38, v4\n"
      "s_mov_b64 exec, s[44:45]\n"
      "ds_add_rtn_u32 v9, v9, v13 offset:0\n"
      "s_mov_b64 exec, s[46:47]\n"
      "ds_add_rtn_u32 v10, v10, v13 offset:0\n"
      "s_mov_b64 exec, s[44:45]\n"
      "s_waitcnt lgkmcnt(1)\n"
      "ds_write_b32 v9, v2\n"
      "s_mov_b64 exec, s[46:47]\n"
      "s_waitcnt lgkmcnt(1)\n"
      "ds_write_b32 v10, v3\n"
      "s_mov_b64 exec, -1\n"
      "s_cbranch_vccnz .Lfg_more\n"
      ".Lfg_ins_done:\n"
      "s_waitcnt vmcnt(0) lgkmcnt(0)\n"
      "s_barrier\n"
      "ds_read_b32 v21, v15 offset:0\n"
      "ds_read_b32 v26, v20 offset:60160\n"
      "ds_read_b32 v27, v20 offset:60224\n"
      "ds_read_b32 v28, v20 offset:60288\n"
      "s_mov_b32 s42, 0\n"
      "s_mov_b32 s43, s3\n"
      "s_waitcnt lgkmcnt(3)\n"
      "v_sub_u32 v21, v21, v14\n"
      "v_lshrrev_b32 v21, 4, v21\n"
      "v_cmp_lt_u32 vcc, 24, v21\n"
      "v_readlane_b32 s44, v21, s3\n"
      "s_cbranch_vccnz .Lfg_slow\n"
      "s_setprio 3\n"
      ".Lfg_node:\n"
      "s_min_u32 s45, s44, 12\n"
      "s_lshl_b32 s46, s45, 4\n"
      "s_sub_u32 s46, s58, s46\n"
      "s_subb_u32 s47, s59, 0\n"
      "s_waitcnt lgkmcnt(0)\n"
      "s_setpc_b64 s[46:47]\n"
      "v_add_u32_dpp v108, v28, v29 quad_perm:[3,3,3,3] row_mask:0xf bank_mask:0xf\n"
      "buffer_load_dwordx4 v[108:111], v108, s[20:23], 0 offen\n"
      "v_add_u32_dpp v104, v28, v29 quad_perm:[2,2,2,2] row_mask:0xf bank_mask:0xf\n"
      "buffer_load_dwordx4 v[104:107], v104, s[20:23], 0 offen\n"
      "v_add_u32_dpp v100, v28, v29 quad_perm:[1,1,1,1] row_mask:0xf bank_mask:0xf\n"
      "buffer_load_dwordx4 v[100:103], v100, s[20:23], 0 offen\n"
      "v_add_u32_dpp v96, v28, v29 quad_perm:[0,0,0,0] row_mask:0xf bank_mask:0xf\n"
      "buffer_load_dwordx4 v[96:99], v96, s[20:23], 0 offen\n"
      "v_add_u32_dpp v92, v27, v29 quad_perm:[3,3,3,3] row_mask:0xf bank_mask:0xf\n"
      "buffer_load_dwordx4 v[92:95], v92, s[20:23], 0 offen\n"
      "v_add_u32_dpp v88, v27, v29 quad_perm:[2,2,2,2] row_mask:0xf bank_mask:0xf\n"
      "buffer_load_dwordx4 v[88:91], v88, s[20:23], 0 offen\n"
      "v_add_u32_dpp v84, v27, v29 quad_perm:[1,1,1,1] row_mask:0xf bank_mask:0xf\n"
      "buffer_load_dwordx4 v[84:87], v84, s[20:23], 0 offen\n"
      "v_add_u32_dpp v80, v27, v29 quad_perm:[0,0,0,0] row_mask:0xf bank_mask:0xf\n"
      "buffer_load_dwordx4 v[80:83], v80, s[20:23], 0 offen\n"
      "v_add_u32_dpp v76, v26, v29 quad_perm:[3,3,3,3] row_mask:0xf bank_mask:0xf\n"
      "buffer_load_dwordx4 v[76:79], v76, s[20:23], 0 offen\n"
      "v_add_u32_dpp v72, v26, v29 quad_perm:[2,2,2,2] row_mask:0xf bank_mask:0xf\n"
      "buffer_load_dwordx4 v[72:75], v72, s[20:23], 0 offen\n"
      "v_add_u32_dpp v68, v26, v29 quad_perm:[1,1,1,1] row_mask:0xf bank_mask:0xf\n"
      "buffer_load_dwordx4 v[68:71], v68, s[20:23], 0 offen\n"
      "v_add_u32_dpp v64, v26, v29 quad_perm:[0,0,0,0] row_mask:0xf bank_mask:0xf\n"
      "buffer_load_dwordx4 v[64:67], v64, s[20:23], 0 offen\n"
      ".Lfg_ldend:\n"
      "s_mov_b64 exec, s[50:51]\n"
      "ds_read_b128 v[48:51], v19 offset:47104\n"
      "s_mov_b64 exec, s[52:53]\n"
      "ds_read_b128 v[52:55], v19 offset:47104\n"
      "s_mov_b64 exec, s[54:55]\n"
      "ds_read_b128 v[56:59], v19 offset:47104\n"
      "s_mov_b64 exec, s[56:57]\n"
      "ds_read_b128 v[60:63], v19 offset:47104\n"
      "s_mov_b64 exec, -1\n"
      "s_sub_u32 s44, s44, s45\n"
      "s_add_u32 s42, s42, 192\n"
      "s_sub_u32 s46, 6336, s42\n"
      "s_cmp_eq_u32 s44, 0\n"
      "s_cselect_b32 s46, s46, 192\n"
      "v_add_u32 v20, s46, v20\n"
      "s_sub_u32 s46, s45, 1\n"
      "s_and_b32 s46, s46, 3\n"
      "v_cmp_lt_u32 vcc, s46, v17\n"
      "ds_read_b32 v26, v20 offset:60160\n"
      "ds_read_b32 v27, v20 offset:60224\n"
      "ds_read_b32 v28, v20 offset:60288\n"
      "v_mov_b32 v40, 0xf149f2ca\n"
      "v_cndmask_b32 v40, 0, v40, vcc\n"
      "s_cmp_gt_u32 s45, 8\n"
      "s_cbranch_scc0 .Lfg_lt3\n"
      ".Lfg_g2:\n"
      "s_waitcnt vmcnt(8) lgkmcnt(3)\n"
      "v_mfma_f32_16x16x32_bf16 v[44:47], v[60:63], v[108:111], v[40:43]\n"
      "v_mfma_f32_16x16x32_bf16 v[44:47], v[56:59], v[104:107], v[44:47]\n"
      "v_mfma_f32_16x16x32_bf16 v[44:47], v[52:55], v[100:103], v[44:47]\n"
      "v_mfma_f32_16x16x32_bf16 v[44:47], v[48:51], v[96:99], v[44:47]\n"
      "v_mov_b32 v40, 0\n"
      "s_nop 6\n"
      "v_add_f32_dpp v44, v45, v44 quad_perm:[1,1,1,1] row_mask:0xf bank_mask:0xf\n"
      "v_add_f32_dpp v46, v47, v46 quad_perm:[3,3,3,3] row_mask:0xf bank_mask:0xf\n"
      "s_nop 1\n"
      "v_add_f32_dpp v44, v46, v44 quad_perm:[2,2,2,2] row_mask:0xf bank_mask:0xf\n"
      "s_nop 1\n"
      "v_mul_f32_dpp v44, v44, v23 quad_perm:[0,0,0,0] row_mask:0xf bank_mask:0xf\n"
      "v_exp_f32 v44, v44\n"
      "s_nop 0\n"
      "v_add_f32 v44, 1.0, v44\n"
      "v_rcp_f32 v44, v44\n"
      "s_nop 0\n"
      "v_cvt_pk_bf16_f32 v44, v44, v44\n"
      "v_and_b32 v30, v44, v24\n"
      "v_and_b32 v31, v44, v25\n"
      "s_nop 1\n"
      "v_mfma_f32_4x4x4_16b_bf16 v[32:35], v[96:97], v[30:31], v[32:35] blgp:4\n"
      "v_mfma_f32_4x4x4_16b_bf16 v[36:39], v[98:99], v[30:31], v[36:39] blgp:4\n"
      "s_nop 0\n"
      "v_mfma_f32_4x4x4_16b_bf16 v[32:35], v[100:101], v[30:31], v[32:35] blgp:5\n"
      "v_mfma_f32_4x4x4_16b_bf16 v[36:39], v[102:103], v[30:31], v[36:39] blgp:5\n"
      "s_nop 0\n"
      "v_mfma_f32_4x4x4_16b_bf16 v[32:35], v[104:105], v[30:31], v[32:35] blgp:6\n"
      "v_mfma_f32_4x4x4_16b_bf16 v[36:39], v[106:107], v[30:31], v[36:39] blgp:6\n"
      "s_nop 0\n"
      "v_mfma_f32_4x4x4_16b_bf16 v[32:35], v[108:109], v[30:31], v[32:35] blgp:7\n"
      "v_mfma_f32_4x4x4_16b_bf16 v[36:39], v[110:111], v[30:31], v[36:39] blgp:7\n"
      ".Lfg_g1:\n"
      "s_waitcnt vmcnt(4) lgkmcnt(3)\n"
      "v_mfma_f32_16x16x32_bf16 v[44:47], v[60:63], v[92:95], v[40:43]\n"
      "v_mfma_f32_16x16x32_bf16 v[44:47], v[56:59], v[88:91], v[44:47]\n"
      "v_mfma_f32_16x16x32_bf16 v[44:47], v[52:55], v[84:87], v[44:47]\n"
      "v_mfma_f32_16x16x32_bf16 v[44:47], v[48:51], v[80:83], v[44:47]\n"
      "v_mov_b32 v40, 0\n"
      "s_nop 6\n"
      "v_add_f32_dpp v44, v45, v44 quad_perm:[1,1,1,1] row_mask:0xf bank_mask:0xf\n"
      "v_add_f32_dpp v46, v47, v46 quad_perm:[3,3,3,3] row_mask:0xf bank_mask:0xf\n"
      "s_nop 1\n"
      "v_add_f32_dpp v44, v46, v44 quad_perm:[2,2,2,2] row_mask:0xf bank_mask:0xf\n"
      "s_nop 1\n"
      "v_mul_f32_dpp v44, v44, v23 quad_perm:[0,0,0,0] row_mask:0xf bank_mask:0xf\n"
      "v_exp_f32 v44, v44\n"
      "s_nop 0\n"
      "v_add_f32 v44, 1.0, v44\n"
      "v_rcp_f32 v44, v44\n"
      "s_nop 0\n"
      "v_cvt_pk_bf16_f32 v44, v44, v44\n"
      "v_and_b32 v30, v44, v24\n"
      "v_and_b32 v31, v44, v25\n"
      "s_nop 1\n"
      "v_mfma_f32_4x4x4_16b_bf16 v[32:35], v[80:81], v[30:31], v[32:35] blgp:4\n"
      "v_mfma_f32_4x4x4_16b_bf16 v[36:39], v[82:83], v[30:31], v[36:39] blgp:4\n"
      "s_nop 0\n"
      "v_mfma_f32_4x4x4_16b_bf16 v[32:35], v[84:85], v[30:31], v[32:35] blgp:5\n"
      "v_mfma_f32_4x4x4_16b_bf16 v[36:39], v[86:87], v[30:31], v[36:39] blgp:5\n"
      "s_nop 0\n"
      "v_mfma_f32_4x4x4_16b_bf16 v[32:35], v[88:89], v[30:31], v[32:35] blgp:6\n"
      "v_mfma_f32_4x4x4_16b_bf16 v[36:39], v[90:91], v[30:31], v[36:39] blgp:6\n"
      "s_nop 0\n"
      "v_mfma_f32_4x4x4_16b_bf16 v[32:35], v[92:93], v[30:31], v[32:35] blgp:7\n"
      "v_mfma_f32_4x4x4_16b_bf16 v[36:39], v[94:95], v[30:31], v[36:39] blgp:7\n"
      ".Lfg_g0:\n"
      "s_waitcnt vmcnt(0) lgkmcnt(3)\n"
      "v_mfma_f32_16x16x32_bf16 v[44:47], v[60:63], v[76:79], v[40:43]\n"
      "v_mfma_f32_16x16x32_bf16 v[44:47], v[56:59], v[72:75], v[44:47]\n"
      "v_mfma_f32_16x16x32_bf16 v[44:47], v[52:55], v[68:71], v[44:47]\n"
      "v_mfma_f32_16x16x32_bf16 v[44:47], v[48:51], v[64:67], v[44:47]\n"
      "v_mov_b32 v40, 0\n"
      "s_nop 6\n"
      "v_add_f32_dpp v44, v45, v44 quad_perm:[1,1,1,1] row_mask:0xf bank_mask:0xf\n"
      "v_add_f32_dpp v46, v47, v46 quad_perm:[3,3,3,3] row_mask:0xf bank_mask:0xf\n"
      "s_nop 1\n"
      "v_add_f32_dpp v44, v46, v44 quad_perm:[2,2,2,2] row_mask:0xf bank_mask:0xf\n"
      "s_nop 1\n"
      "v_mul_f32_dpp v44, v44, v23 quad_perm:[0,0,0,0] row_mask:0xf bank_mask:0xf\n"
      "v_exp_f32 v44, v44\n"
      "s_nop 0\n"
      "v_add_f32 v44, 1.0, v44\n"
      "v_rcp_f32 v44, v44\n"
      "s_nop 0\n"
      "v_cvt_pk_bf16_f32 v44, v44, v44\n"
      "v_and_b32 v30, v44, v24\n"
      "v_and_b32 v31, v44, v25\n"
      "s_nop 1\n"
      "v_mfma_f32_4x4x4_16b_bf16 v[32:35], v[64:65], v[30:31], v[32:35] blgp:4\n"
      "v_mfma_f32_4x4x4_16b_bf16 v[36:39], v[66:67], v[30:31], v[36:39] blgp:4\n"
      "s_nop 0\n"
      "v_mfma_f32_4x4x4_16b_bf16 v[32:35], v[68:69], v[30:31], v[32:35] blgp:5\n"
      "v_mfma_f32_4x4x4_16b_bf16 v[36:39], v[70:71], v[30:31], v[36:39] blgp:5\n"
      "s_nop 0\n"
      "v_mfma_f32_4x4x4_16b_bf16 v[32:35], v[72:73], v[30:31], v[32:35] blgp:6\n"
      "v_mfma_f32_4x4x4_16b_bf16 v[36:39], v[74:75], v[30:31], v[36:39] blgp:6\n"
      "s_nop 0\n"
      "v_mfma_f32_4x4x4_16b_bf16 v[32:35], v[76:77], v[30:31], v[32:35] blgp:7\n"
      "v_mfma_f32_4x4x4_16b_bf16 v[36:39], v[78:79], v[30:31], v[36:39] blgp:7\n"
      "s_cmp_lg_u32 s44, 0\n"
      "s_cbranch_scc1 .Lfg_node\n"
      ".Lfg_flush:\n"
      "s_setprio 2\n"
      "s_cmp_lt_u32 s43, 16\n"
      "s_cbranch_scc1 .Lfg_pr\n"
      "s_setprio 1\n"
      ".Lfg_pr:\n"
      "s_add_u32 s43, s43, 16\n"
      "s_mov_b32 s42, 0\n"
      "v_readlane_b32 s44, v21, s43\n"
      "v_add_u32 v19, 4352, v19\n"
      "v_subrev_u32 v29, 16, v29\n"
      "v_add_f32_dpp v8, v32, v32 row_ror:8 row_mask:0xf bank_mask:0x3\n"
      "v_add_f32_dpp v9, v33, v33 row_ror:8 row_mask:0xf bank_mask:0x3\n"
      "v_add_f32_dpp v8, v34, v34 row_ror:8 row_mask:0xf bank_mask:0xc\n"
      "v_add_f32_dpp v9, v35, v35 row_ror:8 row_mask:0xf bank_mask:0xc\n"
      "v_add_f32_dpp v10, v36, v36 row_ror:8 row_mask:0xf bank_mask:0x3\n"
      "v_add_f32_dpp v11, v37, v37 row_ror:8 row_mask:0xf bank_mask:0x3\n"
      "v_add_f32_dpp v10, v38, v38 row_ror:8 row_mask:0xf bank_mask:0xc\n"
      "v_add_f32_dpp v11, v39, v39 row_ror:8 row_mask:0xf bank_mask:0xc\n"
      "v_add_f32_dpp v2, v8, v8 row_shl:4 row_mask:0xf bank_mask:0x5\n"
      "v_add_f32_dpp v2, v9, v9 row_shr:4 row_mask:0xf bank_mask:0xa\n"
      "v_add_f32_dpp v3, v10, v10 row_shl:4 row_mask:0xf bank_mask:0x5\n"
      "v_add_f32_dpp v3, v11, v11 row_shr:4 row_mask:0xf bank_mask:0xa\n"
      "v_mov_b64 v[32:33], 0\n"
      "v_mov_b64 v[34:35], 0\n"
      "v_mov_b64 v[36:37], 0\n"
      "v_mov_b64 v[38:39], 0\n"
      "v_cvt_pk_bf16_f32 v2, v2, v3\n"
      "s_cmp_lt_u32 s43, 40\n"
      "ds_write_b32 v6, v2\n"
      "v_add_u32 v6, 4352, v6\n"
      "s_cbranch_scc1 .Lfg_node\n"
      "s_cmp_ge_u32 s3, 12\n"
      "s_cbranch_scc1 .Lfg_tail_idle\n"
      "v_bfe_u32 v2, v1, 2, 1\n"
      "v_bfe_u32 v3, v1, 3, 1\n"
      "v_lshl_add_u32 v2, v2, 2, v3\n"
      "v_lshl_add_u32 v21, v2, 8, v16\n"
      "v_add_u32 v21, 104960, v21\n"
      "ds_read_b64_tr_b16 v[64:65], v21 offset:0\n"
      "ds_read_b64_tr_b16 v[66:67], v21 offset:512\n"
      "ds_read_b64_tr_b16 v[68:69], v21 offset:8\n"
      "ds_read_b64_tr_b16 v[70:71], v21 offset:520\n"
      "ds_read_b64_tr_b16 v[72:73], v21 offset:8192\n"
      "ds_read_b64_tr_b16 v[74:75], v21 offset:8704\n"
      "ds_read_b64_tr_b16 v[76:77], v21 offset:8200\n"
      "ds_read_b64_tr_b16 v[78:79], v21 offset:8712\n"
      "ds_read_b64_tr_b16 v[80:81], v21 offset:16384\n"
      "ds_read_b64_tr_b16 v[82:83], v21 offset:16896\n"
      "ds_read_b64_tr_b16 v[84:85], v21 offset:16392\n"
      "ds_read_b64_tr_b16 v[86:87], v21 offset:16904\n"
      "ds_read_b64_tr_b16 v[88:89], v21 offset:24576\n"
      "ds_read_b64_tr_b16 v[90:91], v21 offset:25088\n"
      "ds_read_b64_tr_b16 v[92:93], v21 offset:24584\n"
      "ds_read_b64_tr_b16 v[94:95], v21 offset:25096\n"
      "v_and_b32 v3, 15, v1\n"
      "s_mul_i32 s37, s41, 4352\n"
      "s_add_u32 s37, s37, 91904\n"
      "s_movk_i32 s36, 272\n"
      "v_lshl_add_u32 v22, v17, 4, s37\n"
      "v_mad_u32_u24 v22, v3, s36, v22\n"
      "s_lshl_b32 s36, s41, 20\n"
      "s_lshl_b32 s37, s2, 8\n"
      "s_add_u32 s36, s36, s37\n"
      "s_lshl_b32 s37, s40, 6\n"
      "s_add_u32 s36, s36, s37\n"
      "v_lshl_add_u32 v4, v17, 4, s36\n"
      "v_lshl_add_u32 v4, v3, 16, v4\n"
      "s_waitcnt lgkmcnt(0)\n"
      "s_barrier\n"
      "ds_read_b128 v[112:115], v22 offset:0\n"
      "ds_read_b128 v[116:119], v22 offset:64\n"
      "ds_read_b128 v[120:123], v22 offset:128\n"
      "ds_read_b128 v[124:127], v22 offset:192\n"
      "s_waitcnt lgkmcnt(3)\n"
      "v_mfma_f32_16x16x32_bf16 v[8:11], v[64:67], v[112:115], 0\n"
      "v_mfma_f32_16x16x32_bf16 v[12:15], v[68:71], v[112:115], 0\n"
      "s_waitcnt lgkmcnt(2)\n"
      "v_mfma_f32_16x16x32_bf16 v[8:11], v[72:75], v[116:119], v[8:11]\n"
      "v_mfma_f32_16x16x32_bf16 v[12:15], v[76:79], v[116:119], v[12:15]\n"
      "s_waitcnt lgkmcnt(1)\n"
      "v_mfma_f32_16x16x32_bf16 v[8:11], v[80:83], v[120:123], v[8:11]\n"
      "v_mfma_f32_16x16x32_bf16 v[12:15], v[84:87], v[120:123], v[12:15]\n"
      "s_waitcnt lgkmcnt(0)\n"
      "v_mfma_f32_16x16x32_bf16 v[8:11], v[88:91], v[124:127], v[8:11]\n"
      "v_mfma_f32_16x16x32_bf16 v[12:15], v[92:95], v[124:127], v[12:15]\n"
      "s_nop 6\n"
      "v_cvt_pk_bf16_f32 v8, v8, v9\n"
      "v_cvt_pk_bf16_f32 v9, v10, v11\n"
      "v_cvt_pk_bf16_f32 v10, v12, v13\n"
      "v_cvt_pk_bf16_f32 v11, v14, v15\n"
      "buffer_store_dwordx4 v[8:11], v4, s[28:31], 0 offen \n"
      "s_endpgm\n"
      ".Lfg_tail_idle:\n"
      "s_cmp_lg_u32 s3, 15\n"
      "s_cbranch_scc1 .Lfg_noep\n"
      "s_lshl_b32 s36, s2, 2\n"
      "v_mov_b32 v2, s33\n"
      "s_mov_b64 exec, 1\n"
      "buffer_store_dword v2, off, s[24:27], s36 sc1\n"
      ".Lfg_noep:\n"
      "s_waitcnt lgkmcnt(0)\n"
      "s_barrier\n"
      "s_endpgm\n"
      ".Lfg_lt3:\n"
      "s_cmp_gt_u32 s45, 4\n"
      "s_cbranch_scc1 .Lfg_g1\n"
      "s_cmp_gt_u32 s45, 0\n"
      "s_cbranch_scc1 .Lfg_g0\n"
      "s_branch .Lfg_flush\n"
      ".Lfg_more:\n"
      "v_cmp_ne_u32 s[46:47], s38, v5\n"
      "v_lshlrev_b32_sdwa v9, 2, v4 dst_sel:DWORD dst_unused:UNUSED_PAD src0_sel:DWORD src1_sel:BYTE_0\n"
      "v_lshlrev_b32_sdwa v10, 2, v5 dst_sel:DWORD dst_unused:UNUSED_PAD src0_sel:DWORD src1_sel:BYTE_0\n"
      "s_mov_b64 exec, vcc\n"
      "ds_add_rtn_u32 v9, v9, v13 offset:0\n"
      "s_waitcnt lgkmcnt(0)\n"
      "ds_write_b32 v9, v4\n"
      "s_mov_b64 exec, s[46:47]\n"
      "s_cbranch_execz .Lfg_more_e\n"
      "ds_add_rtn_u32 v10, v10, v13 offset:0\n"
      "s_waitcnt lgkmcnt(0)\n"
      "ds_write_b32 v10, v5\n"
      "s_mov_b32 s36, 0x80808080\n"
      "s_mov_b32 s37, s36\n"
      "s_and_b64 exec, exec, s[36:37]\n"
      "s_cbranch_execz .Lfg_more_e\n"
      ".Lfg_flag:\n"
      "v_mov_b32 v9, 160\n"
      "v_mov_b32 v10, -1\n"
      "ds_write_b32 v9, v10\n"
      ".Lfg_more_e:\n"
      "s_mov_b64 exec, -1\n"
      "s_branch .Lfg_ins_done\n"
      ".Lfg_retry:\n"
      "s_add_u32 s42, s42, 1\n"
      "s_cmp_gt_u32 s42, 0x40000\n"
      "s_cbranch_scc1 .Lfg_flag\n"
      "buffer_load_dwordx2 v[2:3], v18, s[24:27], s39 offen sc1\n"
      "buffer_load_dwordx2 v[4:5], v18, s[24:27], s39 offen offset:64 sc1\n"
      "s_waitcnt vmcnt(0)\n"
      "s_branch .Lfg_chk\n"
      ".Lfg_slow:\n"
      "s_cmp_lg_u32 s3, 15\n"
      "s_cbranch_scc1 .Lfg_end\n"
      "s_lshl_b32 s36, s2, 2\n"
      "v_mov_b32 v2, s33\n"
      "s_mov_b64 exec, 1\n"
      "buffer_store_dword v2, off, s[24:27], s36 sc1\n"
      "s_mov_b64 exec, -1\n"
      ".Lfg_end:\n"
      "s_waitcnt vmcnt(0) lgkmcnt(0)\n"
      :
      : "{s[0:1]}"(__builtin_amdgcn_kernarg_segment_ptr()), "{s2}"(blockIdx.x), "{v0}"(threadIdx.x)
      : "v1", "v2", "v3", "v4", "v5", "v6", "v7", "v8", "v9", "v10", "v11", "v12", "v13", "v14", "v15", "v16", "v17", "v18", "v19", "v20", "v21", "v22", "v23", "v24", "v25", "v26", "v27", "v28", "v29", "v30", "v31", "v32", "v33", "v34", "v35", "v36", "v37", "v38", "v39", "v40", "v41", "v42", "v43", "v44", "v45", "v46", "v47", "v48", "v49", "v50", "v51", "v52", "v53", "v54", "v55", "v56", "v57", "v58", "v59", "v60", "v61", "v62", "v63", "v64", "v65", "v66", "v67", "v68", "v69", "v70", "v71", "v72", "v73", "v74", "v75", "v76", "v77", "v78", "v79", "v80", "v81", "v82", "v83", "v84", "v85", "v86", "v87", "v88", "v89", "v90", "v91", "v92", "v93", "v94", "v95", "v96", "v97", "v98", "v99", "v100", "v101", "v102", "v103", "v104", "v105", "v106", "v107", "v108", "v109", "v110", "v111", "v112", "v113", "v114", "v115", "v116", "v117", "v118", "v119", "v120", "v121", "v122", "v123", "v124", "v125", "v126", "v127", "s3", "s4", "s5", "s6", "s7", "s8", "s9", "s10", "s11", "s12", "s13", "s14", "s15", "s16", "s17", "s18", "s19", "s20", "s21", "s22", "s23", "s24", "s25", "s26", "s27", "s28", "s29", "s30", "s31", "s33", "s34", "s35", "s36", "s37", "s38", "s39", "s40", "s41", "s42", "s43", "s44", "s45", "s46", "s47", "s48", "s49", "s50", "s51", "s52", "s53", "s54", "s55", "s56", "s57", "s58", "s59", "s60", "s61", "s62", "s63", "s64", "s65", "s66", "s67", "s68", "s69", "s70", "s71", "s72", "s73", "s74", "s75", "s76", "s77", "s78", "s79", "s80", "s81", "s82", "s83", "vcc", "scc", "memory");

  char LAS* lds = (char LAS*)smem;
  const int tid = threadIdx.x, lane = tid & 63, b = blockIdx.x;
  const int w = __builtin_amdgcn_readfirstlane(tid >> 6);
  const float kneg = -0.08838834764831845f * 1.4426950408889634f;
  float LAS* ag = (float LAS*)(lds + L_WQ);
  u32 LAS* qu = (u32 LAS*)(lds + L_LIST);
  u32 LAS* qn = (u32 LAS*)(lds + L_CNT);
  for (int i = tid; i < NPB * DD; i += 1024) ag[i] = 0.f;
  for (int base = 0; base < NE; base += 4096) {
    __syncthreads();
    if (tid == 0) *qn = 0;
    __syncthreads();
    const int i = base / 4 + tid;
    if (i < NE / 4) {
      const i32x4 d4 = ((const i32x4*)dst)[i];
#pragma unroll
      for (int j = 0; j < 4; ++j)
        if ((d4[j] & 255) == b) {
          const u32 p = __hip_atomic_fetch_add(qn, 1u, __ATOMIC_RELAXED, __HIP_MEMORY_SCOPE_WORKGROUP);
          qu[p] = ((u32)src[4 * i + j] << 8) | ((u32)d4[j] >> 8);
        }
    }
    __syncthreads();
    const int n = *qn;
    for (int k = w; k < n; k += 16) {
      const u32 rec = qu[k];
      const int nd = rec & 0xFF;
      const u32 xv = *(const u32*)(x + (rec & ~0xFFu) + lane * 4);
      const u32 qv = *(const u32 LAS*)(lds + L_QT + nd * PITCH + lane * 4);
      const float x0 = __builtin_bit_cast(float, xv << 16), x1 = __builtin_bit_cast(float, xv & 0xFFFF0000u);
      float t = x0 * __builtin_bit_cast(float, qv << 16) + x1 * __builtin_bit_cast(float, qv & 0xFFFF0000u);
#pragma unroll
      for (int o = 32; o > 0; o >>= 1) t += __shfl_xor(t, o);
      const float al0 = __builtin_amdgcn_rcpf(1.0f + __builtin_amdgcn_exp2f(t * kneg));
      const float al = __builtin_bit_cast(float, pk_bf16(al0, al0) << 16);
      __hip_atomic_fetch_add(ag + nd * DD + 2 * lane, al * x0, __ATOMIC_RELAXED, __HIP_MEMORY_SCOPE_WORKGROUP);
      __hip_atomic_fetch_add(ag + nd * DD + 2 * lane + 1, al * x1, __ATOMIC_RELAXED, __HIP_MEMORY_SCOPE_WORKGROUP);
    }
  }
  __syncthreads();
  for (int i = tid; i < NPB * 64; i += 1024) {
    const int f = (i >> 6) * DD + ((i & 63) >> 2) * 8 + (i & 3);
    *(u32 LAS*)(lds + L_AG + (i >> 6) * PITCH + (i & 63) * 4) = pk_bf16(ag[f], ag[f + 4]);
  }
  __syncthreads();
  if (w < 12) {
    const int mt = w >> 2, cb = w & 3, g = lane >> 4, q = (lane >> 2) & 3, p = lane & 3;
    const int wa = L_WV + (8 * g + 4 * (q & 1) + (q >> 1)) * 256 + ((4 * (cb ^ q) + p) << 4);
    const int ra = L_AG + (mt * 16 + (lane & 15)) * PITCH + g * 16;
    f32x4 o0 = {0, 0, 0, 0}, o1 = o0;
#pragma unroll
    for (int s = 0; s < 4; ++s) {
      const u32x4 bv = *(const u32x4 LAS*)(lds + ra + s * 64);
      o0 = mfma32(tr_read(lds + wa + s * 8192), tr_read(lds + wa + s * 8192 + 512), bv, o0);
      o1 = mfma32(tr_read(lds + wa + s * 8192 + 8), tr_read(lds + wa + s * 8192 + 520), bv, o1);
    }
    const int node = (mt * 16 + (lane & 15)) * NB + b;
    if (node < NN)
      *(u32x4*)(out + (size_t)node * 256 + (4 * cb + g) * 16) =
          (u32x4){pk_bf16(o0.x, o0.y), pk_bf16(o0.z, o0.w), pk_bf16(o1.x, o1.y), pk_bf16(o1.z, o1.w)};
  }
}

extern "C" void kernel_launch(void* const* d_in, const int* in_sizes, int n_in, void* d_out, int out_size, void* d_ws,
                              size_t ws_size, hipStream_t stream) {
  featgraph_kernel<<<NB, 1024, 0, stream>>>((const char*)d_in[0], (const int*)d_in[1], (const int*)d_in[2], (const char*)d_in[3],
                                            (const char*)d_in[4], (char*)d_ws, (char*)d_out);
}
```

```cpp
#include <hip/hip_runtime.h>
#include <hip/hip_bf16.h>

typedef unsigned u32;
typedef u32 u32x2 __attribute__((ext_vector_type(2)));
typedef u32 u32x4 __attribute__((ext_vector_type(4)));
typedef int i32x4 __attribute__((ext_vector_type(4)));
typedef float f32x4 __attribute__((ext_vector_type(4)));
typedef short s16x4 __attribute__((ext_vector_type(4)));
typedef __bf16 bf16x8 __attribute__((ext_vector_type(8)));
typedef __bf16 bf16x2 __attribute__((ext_vector_type(2)));
typedef float f32x2 __attribute__((ext_vector_type(2)));
#define LAS __attribute__((address_space(3)))
#define GAS __attribute__((address_space(1)))
typedef GAS int gint;
typedef GAS i32x4 gint4;

constexpr size_t WS_OFF = (size_t)0 << 20;
constexpr int NN = 10000, NE = 320000, DD = 128, NB = 256, NPB = 40;
constexpr int PITCH = 272, L_CNT = 0, L_WQ = 59136, L_QT = 1280, L_XD = 14336, L_AG = 42752, L_WV = 92160, L_END = 129024;

__device__ __forceinline__ u32 pk_bf16(float lo, float hi) {
  f32x2 v = {lo, hi};
  return __builtin_bit_cast(u32, __builtin_convertvector(v, bf16x2));
}
__device__ __forceinline__ f32x4 mfma32(u32x2 a0, u32x2 a1, u32x4 b, f32x4 c) {
  const u32x4 a = {a0.x, a0.y, a1.x, a1.y};
  return __builtin_amdgcn_mfma_f32_16x16x32_bf16(__builtin_bit_cast(bf16x8, a), __builtin_bit_cast(bf16x8, b), c, 0, 0, 0);
}
__device__ __forceinline__ u32x2 tr_read(char LAS* p) {
  return __builtin_bit_cast(u32x2, __builtin_amdgcn_ds_read_tr16_b64_v4i16((s16x4 LAS*)p));
}

__global__ __launch_bounds__(1024, 4) void featgraph_kernel(const char* x_, const int* src_, const int* dst_, const char* wq,
                                                            const char* wv, char* ws, char* out_) {
  __shared__ __attribute__((aligned(16))) char smem[L_END];
  typedef const char* const __attribute__((address_space(4))) * karg_t;
  karg_t ka = (karg_t)__builtin_amdgcn_kernarg_segment_ptr();
  asm volatile(
      "s_memrealtime s[18:19]\n"
      "s_load_dwordx8 s[4:11], s[0:1], 0\n"
      "s_load_dwordx4 s[12:15], s[0:1], 32\n"
      "s_load_dwordx2 s[16:17], s[0:1], 48\n"
      "v_readfirstlane_b32 s3, v0\n"
      "v_and_b32 v1, 63, v0\n"
      "v_lshlrev_b32 v18, 4, v0\n"
      "s_lshr_b32 s3, s3, 6\n"
      "v_mov_b32 v8, 4194303\n"
      "v_mov_b32 v9, v8\n"
      "v_mov_b32 v10, v8\n"
      "v_mov_b32 v11, v8\n"
      "v_lshlrev_b32 v15, 2, v1\n"
      "s_movk_i32 s36, 384\n"
      "v_mov_b32 v14, 27392\n"
      "v_mad_u32_u24 v14, v1, s36, v14\n"
      "s_cmp_lg_u32 s3, 0\n"
      "s_cbranch_scc1 .Lfg_nw0\n"
      "s_getpc_b64 s[36:37]\n"
      ".Lfg_pc0:\n"
      "v_lshlrev_b32 v3, 7, v1\n"
      "v_min_u32 v3, .Lfg_end-.Lfg_pc0-4, v3\n"
      "global_load_dword v7, v3, s[36:37]\n"
      "ds_write_b32 v15, v14 offset:0\n"
      ".Lfg_nw0:\n"
      "v_lshrrev_b32 v4, 2, v0\n"
      "s_cmp_ge_u32 s2, 128\n"
      "s_cbranch_scc1 .Lfg_noidx\n"
      "s_movk_i32 s36, 80\n"
      "v_add_u32 v6, 92160, v18\n"
      "v_mad_u32_u24 v6, v4, s36, v6\n"
      "ds_write_b128 v6, v[8:11]\n"
      "s_mul_i32 s48, s2, 10000\n"
      "v_add_u32 v3, s48, v18\n"
      "v_cmp_gt_u32 vcc, 625, v0\n"
      "s_cmp_gt_u32 s3, 3\n"
      "s_mov_b64 s[34:35], vcc\n"
      "s_cbranch_scc1 .Lfg_nocnt\n"
      "s_movk_i32 s36, 144\n"
      "v_mov_b32 v112, 92160\n"
      "v_lshlrev_b32 v113, 2, v0\n"
      "v_mad_u32_u24 v112, v0, s36, v112\n"
      "ds_write_b32 v113, v112 offset:256\n"
      "s_movk_i32 s36, 208\n"
      "v_mad_u32_u24 v114, v4, s36, v18\n"
      "ds_write_b128 v114, v[8:11] offset:1280\n"
      ".Lfg_nocnt:\n"
      "s_mov_b64 exec, s[34:35]\n"
      "s_waitcnt lgkmcnt(0)\n"
      "s_cbranch_execz .Lfg_noidx2\n"
      "global_load_dwordx4 v[96:99], v3, s[8:9]\n"
      "global_load_dwordx4 v[100:103], v3, s[6:7]\n"
      ".Lfg_noidx2:\n"
      "s_mov_b64 exec, -1\n"
      ".Lfg_noidx:\n"
      "ds_write_b128 v18, v[8:11] offset:27392\n"
      "s_lshr_b32 s37, s2, 3\n"
      "s_lshl_b32 s36, s37, 4\n"
      "s_add_u32 s36, s36, s3\n"
      "s_cmp_eq_u32 s36, 250\n"
      "s_waitcnt lgkmcnt(0)\n"
      "s_cbranch_scc1 .Lfg_tok\n"
      "s_load_dword s33, s[14:15], 0\n"
      "s_barrier\n"
      ".Lfg_b1:\n"
      "s_and_b32 s36, s2, 7\n"
      "s_lshl_b32 s36, s36, 5\n"
      "s_or_b32 s48, s36, s37\n"
      "s_and_b32 s40, s3, 3\n"
      "s_mov_b32 s20, s4\n"
      "s_and_b32 s21, s5, 0xffff\n"
      "s_mov_b32 s22, 2560000\n"
      "s_mov_b32 s23, 0x20000\n"
      "s_lshl_b32 s36, s40, 13\n"
      "v_lshl_add_u32 v26, v1, 4, s36\n"
      "s_cmp_lt_u32 s3, 12\n"
      "s_cbranch_scc1 .Lfg_nold\n"
      "global_load_dwordx4 v[64:67], v26, s[10:11] offset:0\n"
      "global_load_dwordx4 v[68:71], v26, s[10:11] offset:1024\n"
      "global_load_dwordx4 v[72:75], v26, s[10:11] offset:2048\n"
      "global_load_dwordx4 v[76:79], v26, s[10:11] offset:3072\n"
      "v_add_u32 v26, 4096, v26\n"
      "global_load_dwordx4 v[80:83], v26, s[10:11] offset:0\n"
      "global_load_dwordx4 v[84:87], v26, s[10:11] offset:1024\n"
      "global_load_dwordx4 v[88:91], v26, s[10:11] offset:2048\n"
      "global_load_dwordx4 v[92:95], v26, s[10:11] offset:3072\n"
      "s_branch .Lfg_ce0\n"
      ".Lfg_nold:\n"
      "s_cmp_lt_u32 s2, 128\n"
      "s_cbranch_scc1 .Lfg_ce0\n"
      "s_cmp_lg_u32 s3, 8\n"
      "s_cbranch_scc1 .Lfg_pf_x\n"
      "s_and_b32 s36, s48, 3\n"
      "s_lshl_b32 s36, s36, 13\n"
      "v_lshl_add_u32 v3, v1, 7, s36\n"
      "global_load_dword v7, v3, s[10:11]\n"
      "global_load_dword v7, v3, s[12:13]\n"
      ".Lfg_pf_x:\n"
      "s_cmp_ge_u32 s3, 4\n"
      "s_cbranch_scc1 .Lfg_ce0\n"
      "s_sleep 2\n"
      "s_and_b32 s36, s48, 15\n"
      "s_mul_i32 s36, s36, 32768\n"
      "v_lshl_add_u32 v3, v0, 7, s36\n"
      "buffer_load_dword v7, v3, s[20:23], 0 offen\n"
      ".Lfg_ce0:\n"
      "v_and_b32 v45, 3, v1\n"
      "v_mov_b32 v13, 4\n"
      "s_mov_b32 s24, s14\n"
      "s_and_b32 s25, s15, 0xffff\n"
      "s_mov_b32 s26, 2641920\n"
      "s_mov_b32 s27, 0x20000\n"
      "s_cmp_ge_u32 s2, 128\n"
      "s_cbranch_scc1 .Lfg_np\n"
      "v_and_b32 v30, 0x70, v18\n"
      "v_lshrrev_b32 v5, 3, v0\n"
      "v_lshrrev_b32 v3, 5, v0\n"
      "v_lshl_add_u32 v30, v5, 14, v30\n"
      "v_xor_b32 v3, s2, v3\n"
      "v_xor_b32 v5, s2, v5\n"
      "v_lshl_add_u32 v28, v5, 7, v30\n"
      "v_lshl_add_u32 v30, v3, 7, v30\n"
      "s_cmp_ge_u32 s3, 10\n"
      "s_cbranch_scc1 .Lfg_nobin\n"
      "v_cmp_lt_u32 vcc, 0, v45\n"
      "v_cmp_lt_u32 s[36:37], 1, v45\n"
      "v_cmp_lt_u32 s[44:45], 2, v45\n"
      "v_mov_b32 v27, 92284\n"
      "v_lshlrev_b32 v3, 4, v45\n"
      "v_cndmask_b32 v116, 0, -1, vcc\n"
      "v_cndmask_b32 v117, 0, -1, s[36:37]\n"
      "v_cndmask_b32 v118, 0, -1, s[44:45]\n"
      "v_sub_u32 v115, v114, v3\n"
      "v_add_u32 v112, 64, v112\n"
      "s_mov_b64 exec, s[34:35]\n"
      "s_mov_b32 s36, 0x0c050401\n"
      "s_waitcnt vmcnt(1) lgkmcnt(0)\n"
      "v_lshlrev_b32_sdwa v104, 2, v96 dst_sel:DWORD dst_unused:UNUSED_PAD src0_sel:DWORD src1_sel:BYTE_0\n"
      "ds_add_rtn_u32 v108, v104, v13 offset:256\n"
      "v_lshlrev_b32_sdwa v105, 2, v97 dst_sel:DWORD dst_unused:UNUSED_PAD src0_sel:DWORD src1_sel:BYTE_0\n"
      "ds_add_rtn_u32 v109, v105, v13 offset:256\n"
      "v_lshlrev_b32_sdwa v106, 2, v98 dst_sel:DWORD dst_unused:UNUSED_PAD src0_sel:DWORD src1_sel:BYTE_0\n"
      "ds_add_rtn_u32 v110, v106, v13 offset:256\n"
      "v_lshlrev_b32_sdwa v107, 2, v99 dst_sel:DWORD dst_unused:UNUSED_PAD src0_sel:DWORD src1_sel:BYTE_0\n"
      "ds_add_rtn_u32 v111, v107, v13 offset:256\n"
      "v_mad_u32_u24 v104, v104, 36, v27\n"
      "v_lshl_or_b32 v96, v96, 14, v96\n"
      "v_mad_u32_u24 v105, v105, 36, v27\n"
      "v_lshl_or_b32 v97, v97, 14, v97\n"
      "v_mad_u32_u24 v106, v106, 36, v27\n"
      "v_lshl_or_b32 v98, v98, 14, v98\n"
      "v_mad_u32_u24 v107, v107, 36, v27\n"
      "v_lshl_or_b32 v99, v99, 14, v99\n"
      "s_waitcnt vmcnt(0)\n"
      "v_perm_b32 v100, v100, v96, s36\n"
      "v_perm_b32 v101, v101, v97, s36\n"
      "v_perm_b32 v102, v102, v98, s36\n"
      "v_perm_b32 v103, v103, v99, s36\n"
      "s_waitcnt lgkmcnt(3)\n"
      "v_min_u32 v108, v108, v104\n"
      "ds_write_b32 v108, v100\n"
      "s_waitcnt lgkmcnt(3)\n"
      "v_min_u32 v109, v109, v105\n"
      "ds_write_b32 v109, v101\n"
      "s_waitcnt lgkmcnt(3)\n"
      "v_min_u32 v110, v110, v106\n"
      "ds_write_b32 v110, v102\n"
      "s_waitcnt lgkmcnt(3)\n"
      "v_min_u32 v111, v111, v107\n"
      "ds_write_b32 v111, v103\n"
      "s_mov_b64 exec, -1\n"
      ".Lfg_nobin:\n"
      "s_movk_i32 s36, 20480\n"
      "s_mov_b32 s37, 2117632\n"
      "s_setprio 2\n"
      "s_waitcnt lgkmcnt(0)\n"
      "s_barrier\n"
      "ds_read_b128 v[8:11], v6\n"
      "s_add_u32 s41, s33, 0x400000\n"
      "s_and_b32 s33, s41, 0xffc00000\n"
      "s_cmp_gt_u32 s3, 3\n"
      "s_cbranch_scc1 .Lfg_co1\n"
      "ds_read_b32 v119, v113 offset:256\n"
      "ds_read_b128 v[120:123], v112\n"
      "s_waitcnt lgkmcnt(1)\n"
      "v_sub_u32 v119, v119, v112\n"
      "v_med3_i32 v119, v119, 0, 64\n"
      "v_or_b32 v8, s33, v8\n"
      "v_or_b32 v9, s33, v9\n"
      "v_or_b32 v10, s33, v10\n"
      "v_or_b32 v11, s33, v11\n"
      "v_and_b32_dpp v116, v119, v116 quad_perm:[0,0,0,0] row_mask:0xf bank_mask:0xf\n"
      "v_and_b32_dpp v117, v119, v117 quad_perm:[1,1,1,1] row_mask:0xf bank_mask:0xf\n"
      "v_and_b32_dpp v118, v119, v118 quad_perm:[2,2,2,2] row_mask:0xf bank_mask:0xf\n"
      "buffer_store_dwordx4 v[8:11], v30, s[24:27], s36 offen sc1\n"
      "v_add3_u32 v116, v116, v117, v118\n"
      "v_add_u32 v115, v115, v116\n"
      ".Lfg_sp:\n"
      "s_waitcnt lgkmcnt(0)\n"
      "v_cmpx_lt_i32 vcc, 0, v119\n"
      "ds_write_b32 v115, v120 offset:1280\n"
      "v_cmpx_lt_i32 vcc, 4, v119\n"
      "ds_write_b32 v115, v121 offset:1284\n"
      "v_cmpx_lt_i32 vcc, 8, v119\n"
      "ds_write_b32 v115, v122 offset:1288\n"
      "v_cmpx_lt_i32 vcc, 12, v119\n"
      "ds_write_b32 v115, v123 offset:1292\n"
      "v_cmpx_lt_i32 vcc, 16, v119\n"
      "s_cbranch_vccnz .Lfg_sp2\n"
      "s_mov_b64 exec, -1\n"
      "ds_read_b128 v[8:11], v114 offset:1280\n"
      "s_mov_b32 s36, s37\n"
      "v_mov_b32 v30, v28\n"
      ".Lfg_co1:\n"
      "s_waitcnt lgkmcnt(0)\n"
      "s_load_dwordx8 s[4:11], s[14:15], 2048\n"
      "v_or_b32 v8, s33, v8\n"
      "v_or_b32 v9, s33, v9\n"
      "v_or_b32 v10, s33, v10\n"
      "v_or_b32 v11, s33, v11\n"
      "buffer_store_dwordx4 v[8:11], v30, s[24:27], s36 offen sc1\n"
      "s_setprio 0\n"
      ".Lfg_consts:\n"
      "v_lshrrev_b32 v44, 4, v1\n"
      "v_and_b32 v45, 3, v1\n"
      "v_bfe_u32 v46, v1, 2, 2\n"
      "v_and_b32 v47, 15, v1\n"
      "v_lshl_add_u32 v22, v44, 2, v45\n"
      "v_lshlrev_b32 v22, 4, v22\n"
      "s_mov_b32 s28, s16\n"
      "s_and_b32 s29, s17, 0xffff\n"
      "s_mov_b32 s30, 2560000\n"
      "s_mov_b32 s31, 0x20000\n"
      "s_sub_u32 s43, 39, s3\n"
      "v_lshl_add_u32 v31, v45, 2, v46\n"
      "s_movk_i32 s36, 27392\n"
      "v_lshl_add_u32 v31, v31, 2, s36\n"
      "s_mul_i32 s36, s43, 384\n"
      "v_add_u32 v20, s36, v31\n"
      "v_subrev_u32 v29, s43, v22\n"
      "v_and_b32 v24, 1, v45\n"
      "v_lshlrev_b32 v24, 4, v24\n"
      "v_mov_b32 v25, 0xffff\n"
      "v_lshlrev_b32 v25, v24, v25\n"
      "v_cmp_gt_u32 vcc, 2, v45\n"
      "v_mov_b32 v23, 0xbe0293ee\n"
      "v_subrev_u32 v14, 12, v14\n"
      "s_nop 0\n"
      "v_cndmask_b32 v24, 0, v25, vcc\n"
      "v_mov_b32 v21, 160\n"
      "v_xor_b32 v25, v25, v24\n"
      "s_movk_i32 s36, 272\n"
      "v_lshlrev_b32 v17, 4, v44\n"
      "v_mad_u32_u24 v17, v47, s36, v17\n"
      "s_mov_b32 s38, 4194303\n"
      "s_mov_b32 s50, 0x000f000f\n"
      "s_mov_b32 s51, s50\n"
      "s_lshl_b64 s[52:53], s[50:51], 4\n"
      "s_lshl_b64 s[54:55], s[50:51], 8\n"
      "s_lshl_b64 s[56:57], s[50:51], 12\n"
      "s_getpc_b64 s[58:59]\n"
      ".Lfg_pc1:\n"
      "s_add_u32 s60, s58, .Lfg_send-.Lfg_pc1\n"
      "s_addc_u32 s61, s59, 0\n"
      "s_add_u32 s62, s58, .Lfg_pvend-.Lfg_pc1\n"
      "s_addc_u32 s63, s59, 0\n"
      "s_add_u32 s58, s58, .Lfg_ldend-.Lfg_pc1\n"
      "s_addc_u32 s59, s59, 0\n"
      "s_lshr_b32 s39, s48, 1\n"
      "s_lshl_b32 s39, s39, 14\n"
      "s_and_b32 s36, s48, 1\n"
      "s_lshl_b32 s36, s36, 6\n"
      "s_add_u32 s39, s39, s36\n"
      "s_add_u32 s39, s39, 20480\n"
      "s_lshr_b32 s49, s48, 3\n"
      "s_lshl_b32 s49, s49, 14\n"
      "s_and_b32 s36, s48, 4\n"
      "s_lshl_b32 s36, s36, 4\n"
      "s_add_u32 s49, s49, s36\n"
      "s_add_u32 s49, s49, 2117632\n"
      "v_lshl_add_u32 v18, v4, 6, v18\n"
      "s_cmp_ge_u32 s3, 12\n"
      "s_cbranch_scc1 .Lfg_cq\n"
      "v_mov_b64 v[48:49], 0\n"
      "v_mov_b64 v[50:51], 0\n"
      "v_mov_b64 v[52:53], 0\n"
      "v_mov_b64 v[54:55], 0\n"
      "v_mov_b64 v[56:57], 0\n"
      "v_mov_b64 v[58:59], 0\n"
      "v_mov_b64 v[60:61], 0\n"
      "v_mov_b64 v[62:63], 0\n"
      "v_mov_b64 v[32:33], 0\n"
      "v_mov_b64 v[34:35], 0\n"
      "v_mov_b64 v[36:37], 0\n"
      "v_mov_b64 v[38:39], 0\n"
      "s_movk_i32 s36, 8256\n"
      "s_lshl_b32 s37, s40, 11\n"
      "s_add_u32 s37, s37, 92160\n"
      "v_lshl_add_u32 v16, v45, 4, s37\n"
      "v_lshl_add_u32 v16, v44, 7, v16\n"
      "v_mad_u32_u24 v16, v46, s36, v16\n"
      "s_branch .Lfg_tk\n"
      ".Lfg_cq:\n"
      "s_movk_i32 s36, 8256\n"
      "s_lshl_b32 s37, s40, 11\n"
      "s_add_u32 s37, s37, 59136\n"
      "v_lshl_add_u32 v12, v45, 4, s37\n"
      "v_lshl_add_u32 v12, v44, 6, v12\n"
      "v_mad_u32_u24 v12, v46, s36, v12\n"
      "s_mul_i32 s36, s40, 8256\n"
      "s_add_u32 s36, s36, 59136\n"
      "v_lshl_add_u32 v16, v47, 4, s36\n"
      "v_lshl_add_u32 v16, v44, 9, v16\n"
      "s_lshl_b32 s36, s40, 18\n"
      "s_lshl_b32 s37, s48, 8\n"
      "s_add_u32 s36, s36, s37\n"
      "v_lshl_add_u32 v19, v47, 4, s36\n"
      "v_lshl_add_u32 v19, v44, 16, v19\n"
      "s_waitcnt vmcnt(1)\n"
      "ds_write_b128 v12, v[64:67] offset:0\n"
      "ds_write_b128 v12, v[68:71] offset:256\n"
      "ds_write_b128 v12, v[72:75] offset:512\n"
      "ds_write_b128 v12, v[76:79] offset:768\n"
      "ds_write_b128 v12, v[80:83] offset:1024\n"
      "ds_write_b128 v12, v[84:87] offset:1280\n"
      "ds_write_b128 v12, v[88:91] offset:1536\n"
      "ds_write_b128 v12, v[92:95] offset:1792\n"
      "s_movk_i32 s36, 272\n"
      "s_mul_i32 s37, s40, 1088\n"
      "v_lshl_add_u32 v12, v47, 4, s37\n"
      "v_mad_u32_u24 v12, v44, s36, v12\n"
      ".Lfg_tk:\n"
      "s_and_b32 s36, s48, 3\n"
      "s_lshl_b32 s36, s36, 6\n"
      "s_or_b32 s16, s36, s33\n"
      "s_xor_b32 s17, s36, s38\n"
      "s_mov_b32 s42, 0\n"
      "s_sub_u32 s18, s18, 500\n"
      ".Lfg_tokc:\n"
      "s_waitcnt lgkmcnt(0)\n"
      "s_sub_u32 s4, s4, s18\n"
      "s_sub_u32 s5, s5, s18\n"
      "s_sub_u32 s6, s6, s18\n"
      "s_sub_u32 s7, s7, s18\n"
      "s_sub_u32 s8, s8, s18\n"
      "s_sub_u32 s9, s9, s18\n"
      "s_sub_u32 s10, s10, s18\n"
      "s_sub_u32 s11, s11, s18\n"
      "s_max_u32 s4, s4, s5\n"
      "s_max_u32 s6, s6, s7\n"
      "s_max_u32 s8, s8, s9\n"
      "s_max_u32 s10, s10, s11\n"
      "s_max_u32 s4, s4, s6\n"
      "s_max_u32 s8, s8, s10\n"
      "s_max_u32 s4, s4, s8\n"
      "s_cmp_gt_u32 s4, 5000\n"
      "s_cbranch_scc1 .Lfg_tokr\n"
      "s_add_u32 s18, s18, s4\n"
      ".Lfg_toke:\n"
      "s_mov_b32 s42, 0\n"
      "s_add_u32 s19, s18, 140\n"
      "s_branch .Lfg_dl\n"
      ".Lfg_dls:\n"
      "s_sleep 1\n"
      ".Lfg_dl:\n"
      "s_memrealtime s[36:37]\n"
      "s_waitcnt lgkmcnt(0)\n"
      "s_sub_u32 s36, s36, s19\n"
      "s_cmp_lt_i32 s36, 0\n"
      "s_cbranch_scc1 .Lfg_dls\n"
      "s_cmp_ge_u32 s3, 8\n"
      "s_cbranch_scc1 .Lfg_rl\n"
      "buffer_load_dwordx4 v[2:5], v18, s[24:27], s39 offen sc1\n"
      "s_sleep 1\n"
      "buffer_load_dwordx4 v[8:11], v18, s[24:27], s49 offen sc1\n"
      "s_mov_b32 s4, 0x88888888\n"
      "s_mov_b32 s5, s4\n"
      "s_waitcnt vmcnt(1)\n"
      ".Lfg_chk:\n"
      "v_xor_b32 v2, s16, v2\n"
      "v_xor_b32 v3, s16, v3\n"
      "v_xor_b32 v4, s16, v4\n"
      "v_xor_b32 v5, s16, v5\n"
      "s_mov_b64 exec, -1\n"
      "v_or3_b32 v6, v2, v3, v4\n"
      "v_lshlrev_b32_sdwa v26, 2, v2 dst_sel:DWORD dst_unused:UNUSED_PAD src0_sel:DWORD src1_sel:BYTE_0\n"
      "v_or_b32 v6, v6, v5\n"
      "v_lshlrev_b32_sdwa v27, 2, v3 dst_sel:DWORD dst_unused:UNUSED_PAD src0_sel:DWORD src1_sel:BYTE_0\n"
      "v_cmp_lt_u32 vcc, s38, v6\n"
      "v_lshlrev_b32_sdwa v28, 2, v4 dst_sel:DWORD dst_unused:UNUSED_PAD src0_sel:DWORD src1_sel:BYTE_0\n"
      "v_lshlrev_b32_sdwa v30, 2, v5 dst_sel:DWORD dst_unused:UNUSED_PAD src0_sel:DWORD src1_sel:BYTE_0\n"
      "s_cbranch_vccnz .Lfg_retry\n"
      "v_cmpx_lt_u32 s[44:45], v26, v21\n"
      "ds_add_rtn_u32 v26, v26, v13 offset:0\n"
      "v_cmpx_lt_u32 s[46:47], v27, v21\n"
      "ds_add_rtn_u32 v27, v27, v13 offset:0\n"
      "v_cmpx_lt_u32 s[34:35], v28, v21\n"
      "ds_add_rtn_u32 v28, v28, v13 offset:0\n"
      "v_cmpx_lt_u32 s[36:37], v30, v21\n"
      "ds_add_rtn_u32 v30, v30, v13 offset:0\n"
      "s_and_b64 vcc, s[36:37], s[4:5]\n"
      "s_cbranch_scc1 .Lfg_dob\n"
      "s_waitcnt lgkmcnt(0)\n"
      ".Lfg_puta:\n"
      "s_mov_b64 exec, s[36:37]\n"
      "ds_write_b32 v30, v5\n"
      "s_mov_b64 exec, s[34:35]\n"
      "ds_write_b32 v28, v4\n"
      "s_mov_b64 exec, s[46:47]\n"
      "ds_write_b32 v27, v3\n"
      "s_mov_b64 exec, s[44:45]\n"
      "ds_write_b32 v26, v2\n"
      ".Lfg_b4x:\n"
      "s_mov_b64 exec, -1\n"
      "s_setprio 3\n"
      "s_waitcnt vmcnt(0) lgkmcnt(0)\n"
      "s_barrier\n"
      ".Lfg_go:\n"
      "ds_read_b32 v21, v15 offset:0\n"
      "ds_read_b32 v26, v20 offset:0\n"
      "ds_read_b32 v27, v20 offset:64\n"
      "ds_read_b32 v28, v20 offset:128\n"
      "ds_read_b32 v30, v20 offset:192\n"
      "s_mov_b32 s10, 1\n"
      "s_waitcnt lgkmcnt(4)\n"
      "v_sub_u32 v21, v21, v14\n"
      "v_lshrrev_b32 v21, 4, v21\n"
      "v_cmp_lt_u32 vcc, 24, v21\n"
      "v_readlane_b32 s44, v21, s43\n"
      "s_cbranch_vccnz .Lfg_slow\n"
      "s_min_u32 s45, s44, 16\n"
      "s_lshl_b32 s46, s45, 4\n"
      "s_sub_u32 s4, s58, s46\n"
      "s_subb_u32 s5, s59, 0\n"
      ".Lfg_node:\n"
      "s_waitcnt lgkmcnt(0)\n"
      "s_setpc_b64 s[4:5]\n"
      "v_add_u32_dpp v124, v30, v29 quad_perm:[3,3,3,3] row_mask:0xf bank_mask:0xf\n"
      "buffer_load_dwordx4 v[124:127], v124, s[20:23], 0 offen\n"
      "v_add_u32_dpp v120, v30, v29 quad_perm:[2,2,2,2] row_mask:0xf bank_mask:0xf\n"
      "buffer_load_dwordx4 v[120:123], v120, s[20:23], 0 offen\n"
      "v_add_u32_dpp v116, v30, v29 quad_perm:[1,1,1,1] row_mask:0xf bank_mask:0xf\n"
      "buffer_load_dwordx4 v[116:119], v116, s[20:23], 0 offen\n"
      "v_add_u32_dpp v112, v30, v29 quad_perm:[0,0,0,0] row_mask:0xf bank_mask:0xf\n"
      "buffer_load_dwordx4 v[112:115], v112, s[20:23], 0 offen\n"
      "v_add_u32_dpp v108, v28, v29 quad_perm:[3,3,3,3] row_mask:0xf bank_mask:0xf\n"
      "buffer_load_dwordx4 v[108:111], v108, s[20:23], 0 offen\n"
      "v_add_u32_dpp v104, v28, v29 quad_perm:[2,2,2,2] row_mask:0xf bank_mask:0xf\n"
      "buffer_load_dwordx4 v[104:107], v104, s[20:23], 0 offen\n"
      "v_add_u32_dpp v100, v28, v29 quad_perm:[1,1,1,1] row_mask:0xf bank_mask:0xf\n"
      "buffer_load_dwordx4 v[100:103], v100, s[20:23], 0 offen\n"
      "v_add_u32_dpp v96, v28, v29 quad_perm:[0,0,0,0] row_mask:0xf bank_mask:0xf\n"
      "buffer_load_dwordx4 v[96:99], v96, s[20:23], 0 offen\n"
      "v_add_u32_dpp v92, v27, v29 quad_perm:[3,3,3,3] row_mask:0xf bank_mask:0xf\n"
      "buffer_load_dwordx4 v[92:95], v92, s[20:23], 0 offen\n"
      "v_add_u32_dpp v88, v27, v29 quad_perm:[2,2,2,2] row_mask:0xf bank_mask:0xf\n"
      "buffer_load_dwordx4 v[88:91], v88, s[20:23], 0 offen\n"
      "v_add_u32_dpp v84, v27, v29 quad_perm:[1,1,1,1] row_mask:0xf bank_mask:0xf\n"
      "buffer_load_dwordx4 v[84:87], v84, s[20:23], 0 offen\n"
      "v_add_u32_dpp v80, v27, v29 quad_perm:[0,0,0,0] row_mask:0xf bank_mask:0xf\n"
      "buffer_load_dwordx4 v[80:83], v80, s[20:23], 0 offen\n"
      "v_add_u32_dpp v76, v26, v29 quad_perm:[3,3,3,3] row_mask:0xf bank_mask:0xf\n"
      "buffer_load_dwordx4 v[76:79], v76, s[20:23], 0 offen\n"
      "v_add_u32_dpp v72, v26, v29 quad_perm:[2,2,2,2] row_mask:0xf bank_mask:0xf\n"
      "buffer_load_dwordx4 v[72:75], v72, s[20:23], 0 offen\n"
      "v_add_u32_dpp v68, v26, v29 quad_perm:[1,1,1,1] row_mask:0xf bank_mask:0xf\n"
      "buffer_load_dwordx4 v[68:71], v68, s[20:23], 0 offen\n"
      "v_add_u32_dpp v64, v26, v29 quad_perm:[0,0,0,0] row_mask:0xf bank_mask:0xf\n"
      "buffer_load_dwordx4 v[64:67], v64, s[20:23], 0 offen\n"
      ".Lfg_ldend:\n"
      "s_sub_u32 s44, s44, s45\n"
      "s_or_b32 s6, s44, s10\n"
      "s_cbranch_scc1 .Lfg_rare\n"
      ".Lfg_tick:\n"
      "v_mov_b32 v14, 0\n"
      "s_mov_b64 exec, 1\n"
      "ds_add_rtn_u32 v14, v14, v13 offset:236\n"
      "s_mov_b64 exec, -1\n"
      ".Lfg_qrd:\n"
      "s_mul_i32 s6, s43, 272\n"
      "s_cmp_ge_u32 s43, 16\n"
      "s_cselect_b32 s7, 4, 0\n"
      "v_add_u32 v19, s6, v22\n"
      "s_mov_b64 exec, s[56:57]\n"
      "ds_read_b128 v[60:63], v19 offset:1280\n"
      "s_mov_b64 exec, s[54:55]\n"
      "ds_read_b128 v[56:59], v19 offset:1280\n"
      "s_mov_b64 exec, s[52:53]\n"
      "ds_read_b128 v[52:55], v19 offset:1280\n"
      "s_mov_b64 exec, s[50:51]\n"
      "ds_read_b128 v[48:51], v19 offset:1280\n"
      "s_mov_b64 exec, -1\n"
      "v_lshl_add_u32 v6, v1, 2, s6\n"
      "v_mov_b32 v15, s7\n"
      "s_mul_i32 s46, s45, 12\n"
      "s_sub_u32 s46, s60, s46\n"
      "s_subb_u32 s47, s61, 0\n"
      "s_mul_i32 s34, s45, 20\n"
      "s_sub_u32 s34, s62, s34\n"
      "s_subb_u32 s35, s63, 0\n"
      "s_mov_b32 s8, s45\n"
      "v_mov_b64 v[40:41], 0\n"
      "v_mov_b64 v[42:43], 0\n"
      "v_mov_b64 v[44:45], 0\n"
      "v_mov_b64 v[46:47], 0\n"
      "v_mov_b64 v[2:3], 0\n"
      "v_mov_b64 v[4:5], 0\n"
      "v_mov_b64 v[8:9], 0\n"
      "v_mov_b64 v[10:11], 0\n"
      "s_cmp_lg_u32 s44, 0\n"
      "s_cselect_b64 vcc, -1, 0\n"
      "s_cbranch_scc1 .Lfg_cont\n"
      "s_waitcnt lgkmcnt(4)\n"
      "v_readfirstlane_b32 s43, v14\n"
      "s_sub_u32 s43, 50140, s43\n"
      "s_ashr_i32 s43, s43, 2\n"
      "s_max_i32 s7, s43, 0\n"
      "s_mul_i32 s6, s7, 384\n"
      "v_readlane_b32 s44, v21, s7\n"
      "v_add_u32 v20, s6, v31\n"
      "v_subrev_u32 v29, s43, v22\n"
      ".Lfg_ent:\n"
      "ds_read_b32 v26, v20 offset:0\n"
      "ds_read_b32 v27, v20 offset:64\n"
      "ds_read_b32 v28, v20 offset:128\n"
      "ds_read_b32 v30, v20 offset:192\n"
      "s_min_u32 s45, s44, 16\n"
      "s_lshl_b32 s6, s45, 4\n"
      "s_sub_u32 s4, s58, s6\n"
      "s_subb_u32 s5, s59, 0\n"
      "s_cmp_gt_u32 s8, 8\n"
      "s_waitcnt lgkmcnt(4)\n"
      "s_setpc_b64 s[46:47]\n"
      "s_waitcnt vmcnt(15)\n"
      "v_mfma_f32_16x16x32_bf16 v[8:11], v[60:63], v[124:127], v[8:11]\n"
      "s_waitcnt vmcnt(14)\n"
      "v_mfma_f32_16x16x32_bf16 v[8:11], v[56:59], v[120:123], v[8:11]\n"
      "s_waitcnt vmcnt(13)\n"
      "v_mfma_f32_16x16x32_bf16 v[8:11], v[52:55], v[116:119], v[8:11]\n"
      "s_waitcnt vmcnt(12)\n"
      "v_mfma_f32_16x16x32_bf16 v[8:11], v[48:51], v[112:115], v[8:11]\n"
      "s_waitcnt vmcnt(11)\n"
      "v_mfma_f32_16x16x32_bf16 v[2:5], v[60:63], v[108:111], v[2:5]\n"
      "s_waitcnt vmcnt(10)\n"
      "v_mfma_f32_16x16x32_bf16 v[2:5], v[56:59], v[104:107], v[2:5]\n"
      "s_waitcnt vmcnt(9)\n"
      "v_mfma_f32_16x16x32_bf16 v[2:5], v[52:55], v[100:103], v[2:5]\n"
      "s_waitcnt vmcnt(8)\n"
      "v_mfma_f32_16x16x32_bf16 v[2:5], v[48:51], v[96:99], v[2:5]\n"
      "s_waitcnt vmcnt(7)\n"
      "v_mfma_f32_16x16x32_bf16 v[44:47], v[60:63], v[92:95], v[44:47]\n"
      "s_waitcnt vmcnt(6)\n"
      "v_mfma_f32_16x16x32_bf16 v[44:47], v[56:59], v[88:91], v[44:47]\n"
      "s_waitcnt vmcnt(5)\n"
      "v_mfma_f32_16x16x32_bf16 v[44:47], v[52:55], v[84:87], v[44:47]\n"
      "s_waitcnt vmcnt(4)\n"
      "v_mfma_f32_16x16x32_bf16 v[44:47], v[48:51], v[80:83], v[44:47]\n"
      "s_waitcnt vmcnt(3)\n"
      "v_mfma_f32_16x16x32_bf16 v[40:43], v[60:63], v[76:79], v[40:43]\n"
      "s_waitcnt vmcnt(2)\n"
      "v_mfma_f32_16x16x32_bf16 v[40:43], v[56:59], v[72:75], v[40:43]\n"
      "s_waitcnt vmcnt(1)\n"
      "v_mfma_f32_16x16x32_bf16 v[40:43], v[52:55], v[68:71], v[40:43]\n"
      "s_waitcnt vmcnt(0)\n"
      "v_mfma_f32_16x16x32_bf16 v[40:43], v[48:51], v[64:67], v[40:43]\n"
      ".Lfg_send:\n"
      "s_cbranch_scc0 .Lfg_sig2\n"
      "s_cmp_gt_u32 s8, 12\n"
      "s_cbranch_scc1 .Lfg_sig4\n"
      ".Lfg_sig3:\n"
      "v_add_f32_dpp v2, v3, v2 quad_perm:[1,1,1,1] row_mask:0xf bank_mask:0xf\n"
      "v_add_f32_dpp v4, v5, v4 quad_perm:[3,3,3,3] row_mask:0xf bank_mask:0xf\n"
      "v_add_f32_dpp v44, v45, v44 quad_perm:[1,1,1,1] row_mask:0xf bank_mask:0xf\n"
      "v_add_f32_dpp v46, v47, v46 quad_perm:[3,3,3,3] row_mask:0xf bank_mask:0xf\n"
      "v_add_f32_dpp v2, v4, v2 quad_perm:[2,2,2,2] row_mask:0xf bank_mask:0xf\n"
      "s_nop 0\n"
      "v_add_f32_dpp v44, v46, v44 quad_perm:[2,2,2,2] row_mask:0xf bank_mask:0xf\n"
      "v_add_f32_dpp v40, v41, v40 quad_perm:[1,1,1,1] row_mask:0xf bank_mask:0xf\n"
      "v_add_f32_dpp v42, v43, v42 quad_perm:[3,3,3,3] row_mask:0xf bank_mask:0xf\n"
      "v_mul_f32_dpp v2, v2, v23 quad_perm:[0,0,0,0] row_mask:0xf bank_mask:0xf\n"
      "v_mul_f32_dpp v44, v44, v23 quad_perm:[0,0,0,0] row_mask:0xf bank_mask:0xf\n"
      "v_add_f32_dpp v40, v42, v40 quad_perm:[2,2,2,2] row_mask:0xf bank_mask:0xf\n"
      "v_exp_f32 v2, v2\n"
      "v_exp_f32 v44, v44\n"
      "v_mul_f32_dpp v40, v40, v23 quad_perm:[0,0,0,0] row_mask:0xf bank_mask:0xf\n"
      "v_add_f32 v2, 1.0, v2\n"
      "v_exp_f32 v40, v40\n"
      "v_add_f32 v44, 1.0, v44\n"
      "v_rcp_f32 v2, v2\n"
      "v_add_f32 v40, 1.0, v40\n"
      "v_rcp_f32 v44, v44\n"
      "v_cvt_pk_bf16_f32 v2, v2, v2\n"
      "v_rcp_f32 v40, v40\n"
      "v_cvt_pk_bf16_f32 v44, v44, v44\n"
      "v_and_b32 v4, v2, v24\n"
      "v_and_b32 v5, v2, v25\n"
      "v_cvt_pk_bf16_f32 v40, v40, v40\n"
      "v_and_b32 v46, v44, v24\n"
      "v_and_b32 v47, v44, v25\n"
      "v_and_b32 v42, v40, v24\n"
      "v_and_b32 v43, v40, v25\n"
      "s_setpc_b64 s[34:35]\n"
      ".Lfg_sig2:\n"
      "s_nop 2\n"
      "v_add_f32_dpp v44, v45, v44 quad_perm:[1,1,1,1] row_mask:0xf bank_mask:0xf\n"
      "v_add_f32_dpp v46, v47, v46 quad_perm:[3,3,3,3] row_mask:0xf bank_mask:0xf\n"
      "s_nop 1\n"
      "v_add_f32_dpp v44, v46, v44 quad_perm:[2,2,2,2] row_mask:0xf bank_mask:0xf\n"
      "v_add_f32_dpp v40, v41, v40 quad_perm:[1,1,1,1] row_mask:0xf bank_mask:0xf\n"
      "v_add_f32_dpp v42, v43, v42 quad_perm:[3,3,3,3] row_mask:0xf bank_mask:0xf\n"
      "v_mul_f32_dpp v44, v44, v23 quad_perm:[0,0,0,0] row_mask:0xf bank_mask:0xf\n"
      "v_exp_f32 v44, v44\n"
      "v_add_f32_dpp v40, v42, v40 quad_perm:[2,2,2,2] row_mask:0xf bank_mask:0xf\n"
      "v_add_f32 v44, 1.0, v44\n"
      "v_rcp_f32 v44, v44\n"
      "v_mul_f32_dpp v40, v40, v23 quad_perm:[0,0,0,0] row_mask:0xf bank_mask:0xf\n"
      "v_exp_f32 v40, v40\n"
      "v_cvt_pk_bf16_f32 v44, v44, v44\n"
      "v_add_f32 v40, 1.0, v40\n"
      "v_rcp_f32 v40, v40\n"
      "v_and_b32 v46, v44, v24\n"
      "v_and_b32 v47, v44, v25\n"
      "v_cvt_pk_bf16_f32 v40, v40, v40\n"
      "v_and_b32 v42, v40, v24\n"
      "v_and_b32 v43, v40, v25\n"
      "s_nop 0\n"
      "s_setpc_b64 s[34:35]\n"
      "v_mfma_f32_4x4x4_16b_bf16 v[32:35], v[124:125], v[10:11], v[32:35] blgp:7\n"
      "v_mfma_f32_4x4x4_16b_bf16 v[36:39], v[126:127], v[10:11], v[36:39] blgp:7\n"
      "s_nop 0\n"
      "v_mfma_f32_4x4x4_16b_bf16 v[32:35], v[120:121], v[10:11], v[32:35] blgp:6\n"
      "v_mfma_f32_4x4x4_16b_bf16 v[36:39], v[122:123], v[10:11], v[36:39] blgp:6\n"
      "s_nop 0\n"
      "v_mfma_f32_4x4x4_16b_bf16 v[32:35], v[116:117], v[10:11], v[32:35] blgp:5\n"
      "v_mfma_f32_4x4x4_16b_bf16 v[36:39], v[118:119], v[10:11], v[36:39] blgp:5\n"
      "s_nop 0\n"
      "v_mfma_f32_4x4x4_16b_bf16 v[32:35], v[112:113], v[10:11], v[32:35] blgp:4\n"
      "v_mfma_f32_4x4x4_16b_bf16 v[36:39], v[114:115], v[10:11], v[36:39] blgp:4\n"
      "s_nop 0\n"
      "v_mfma_f32_4x4x4_16b_bf16 v[32:35], v[108:109], v[4:5], v[32:35] blgp:7\n"
      "v_mfma_f32_4x4x4_16b_bf16 v[36:39], v[110:111], v[4:5], v[36:39] blgp:7\n"
      "s_nop 0\n"
      "v_mfma_f32_4x4x4_16b_bf16 v[32:35], v[104:105], v[4:5], v[32:35] blgp:6\n"
      "v_mfma_f32_4x4x4_16b_bf16 v[36:39], v[106:107], v[4:5], v[36:39] blgp:6\n"
      "s_nop 0\n"
      "v_mfma_f32_4x4x4_16b_bf16 v[32:35], v[100:101], v[4:5], v[32:35] blgp:5\n"
      "v_mfma_f32_4x4x4_16b_bf16 v[36:39], v[102:103], v[4:5], v[36:39] blgp:5\n"
      "s_nop 0\n"
      "v_mfma_f32_4x4x4_16b_bf16 v[32:35], v[96:97], v[4:5], v[32:35] blgp:4\n"
      "v_mfma_f32_4x4x4_16b_bf16 v[36:39], v[98:99], v[4:5], v[36:39] blgp:4\n"
      "s_nop 0\n"
      "v_mfma_f32_4x4x4_16b_bf16 v[32:35], v[92:93], v[46:47], v[32:35] blgp:7\n"
      "v_mfma_f32_4x4x4_16b_bf16 v[36:39], v[94:95], v[46:47], v[36:39] blgp:7\n"
      "s_nop 0\n"
      "v_mfma_f32_4x4x4_16b_bf16 v[32:35], v[88:89], v[46:47], v[32:35] blgp:6\n"
      "v_mfma_f32_4x4x4_16b_bf16 v[36:39], v[90:91], v[46:47], v[36:39] blgp:6\n"
      "s_nop 0\n"
      "v_mfma_f32_4x4x4_16b_bf16 v[32:35], v[84:85], v[46:47], v[32:35] blgp:5\n"
      "v_mfma_f32_4x4x4_16b_bf16 v[36:39], v[86:87], v[46:47], v[36:39] blgp:5\n"
      "s_nop 0\n"
      "v_mfma_f32_4x4x4_16b_bf16 v[32:35], v[80:81], v[46:47], v[32:35] blgp:4\n"
      "v_mfma_f32_4x4x4_16b_bf16 v[36:39], v[82:83], v[46:47], v[36:39] blgp:4\n"
      "s_nop 0\n"
      "v_mfma_f32_4x4x4_16b_bf16 v[32:35], v[76:77], v[42:43], v[32:35] blgp:7\n"
      "v_mfma_f32_4x4x4_16b_bf16 v[36:39], v[78:79], v[42:43], v[36:39] blgp:7\n"
      "s_nop 0\n"
      "v_mfma_f32_4x4x4_16b_bf16 v[32:35], v[72:73], v[42:43], v[32:35] blgp:6\n"
      "v_mfma_f32_4x4x4_16b_bf16 v[36:39], v[74:75], v[42:43], v[36:39] blgp:6\n"
      "s_nop 0\n"
      "v_mfma_f32_4x4x4_16b_bf16 v[32:35], v[68:69], v[42:43], v[32:35] blgp:5\n"
      "v_mfma_f32_4x4x4_16b_bf16 v[36:39], v[70:71], v[42:43], v[36:39] blgp:5\n"
      "s_nop 0\n"
      "v_mfma_f32_4x4x4_16b_bf16 v[32:35], v[64:65], v[42:43], v[32:35] blgp:4\n"
      "v_mfma_f32_4x4x4_16b_bf16 v[36:39], v[66:67], v[42:43], v[36:39] blgp:4\n"
      "s_nop 0\n"
      ".Lfg_pvend:\n"
      "s_cbranch_vccnz .Lfg_node\n"
      "s_cmp_lt_i32 s43, 0\n"
      "s_nop 0\n"
      "v_add_f32_dpp v8, v32, v32 row_ror:8 row_mask:0xf bank_mask:0x3\n"
      "v_add_f32_dpp v9, v33, v33 row_ror:8 row_mask:0xf bank_mask:0x3\n"
      "v_add_f32_dpp v10, v36, v36 row_ror:8 row_mask:0xf bank_mask:0x3\n"
      "v_add_f32_dpp v11, v37, v37 row_ror:8 row_mask:0xf bank_mask:0x3\n"
      "v_add_f32_dpp v8, v34, v34 row_ror:8 row_mask:0xf bank_mask:0xc\n"
      "v_add_f32_dpp v10, v38, v38 row_ror:8 row_mask:0xf bank_mask:0xc\n"
      "v_add_f32_dpp v9, v35, v35 row_ror:8 row_mask:0xf bank_mask:0xc\n"
      "v_add_f32_dpp v11, v39, v39 row_ror:8 row_mask:0xf bank_mask:0xc\n"
      "v_add_f32_dpp v2, v8, v8 row_shl:4 row_mask:0xf bank_mask:0x5\n"
      "v_add_f32_dpp v3, v10, v10 row_shl:4 row_mask:0xf bank_mask:0x5\n"
      "v_add_f32_dpp v2, v9, v9 row_shr:4 row_mask:0xf bank_mask:0xa\n"
      "v_add_f32_dpp v3, v11, v11 row_shr:4 row_mask:0xf bank_mask:0xa\n"
      "v_cvt_pk_bf16_f32 v2, v2, v3\n"
      "ds_write_b32 v6, v2 offset:42752\n"
      "s_mov_b64 exec, 1\n"
      "ds_add_u32 v15, v13 offset:244\n"
      "s_mov_b64 exec, -1\n"
      "s_cbranch_scc1 .Lfg_tail\n"
      "v_mov_b64 v[32:33], 0\n"
      "v_mov_b64 v[34:35], 0\n"
      "v_mov_b64 v[36:37], 0\n"
      "v_mov_b64 v[38:39], 0\n"
      "s_setprio 2\n"
      "s_cmp_ge_u32 s43, 16\n"
      "s_cbranch_scc1 .Lfg_node\n"
      "s_setprio 1\n"
      "s_branch .Lfg_node\n"
      ".Lfg_tail:\n"
      "s_not_b32 s40, s43\n"
      "s_cmp_lt_u32 s40, 5\n"
      "s_cbranch_scc1 .Lfg_t2\n"
      ".Lfg_fin:\n"
      "s_waitcnt lgkmcnt(0)\n"
      "s_barrier\n"
      ".Lfg_done:\n"
      "s_endpgm\n"
      ".Lfg_t2:\n"
      "s_cmp_eq_u32 s40, 4\n"
      "s_cbranch_scc1 .Lfg_bump\n"
      "s_mul_i32 s36, s40, 8256\n"
      "s_add_u32 s36, s36, 92160\n"
      "v_and_b32 v4, 15, v1\n"
      "v_and_b32 v3, 48, v1\n"
      "v_lshl_add_u32 v16, v4, 4, s36\n"
      "v_lshl_add_u32 v16, v3, 5, v16\n"
      "s_mov_b32 s11, 2\n"
      "v_add_u32 v17, 8704, v17\n"
      "s_lshl_b32 s36, s48, 8\n"
      "s_lshl_b32 s37, s40, 6\n"
      "s_add_u32 s36, s36, s37\n"
      "s_add_u32 s36, s36, 2097152\n"
      "v_add_u32 v3, s36, v3\n"
      "v_lshl_add_u32 v4, v4, 16, v3\n"
      "v_mov_b32 v5, 248\n"
      ".Lfg_spin:\n"
      "ds_read_b32 v3, v5\n"
      "s_waitcnt lgkmcnt(0)\n"
      "v_readfirstlane_b32 s36, v3\n"
      "s_cmp_lg_u32 s36, 51296\n"
      "s_cbranch_scc0 .Lfg_wf\n"
      "s_sleep 1\n"
      "s_branch .Lfg_spin\n"
      ".Lfg_wf:\n"
      "ds_read_b64_tr_b16 v[64:65], v16 offset:0\n"
      "ds_read_b64_tr_b16 v[66:67], v16 offset:256\n"
      "ds_read_b64_tr_b16 v[68:69], v16 offset:8\n"
      "ds_read_b64_tr_b16 v[70:71], v16 offset:264\n"
      "ds_read_b64_tr_b16 v[72:73], v16 offset:2048\n"
      "ds_read_b64_tr_b16 v[74:75], v16 offset:2304\n"
      "ds_read_b64_tr_b16 v[76:77], v16 offset:2056\n"
      "ds_read_b64_tr_b16 v[78:79], v16 offset:2312\n"
      "ds_read_b64_tr_b16 v[80:81], v16 offset:4096\n"
      "ds_read_b64_tr_b16 v[82:83], v16 offset:4352\n"
      "ds_read_b64_tr_b16 v[84:85], v16 offset:4104\n"
      "ds_read_b64_tr_b16 v[86:87], v16 offset:4360\n"
      "ds_read_b64_tr_b16 v[88:89], v16 offset:6144\n"
      "ds_read_b64_tr_b16 v[90:91], v16 offset:6400\n"
      "ds_read_b64_tr_b16 v[92:93], v16 offset:6152\n"
      "ds_read_b64_tr_b16 v[94:95], v16 offset:6408\n"
      ".Lfg_proj:\n"
      "ds_read_b128 v[112:115], v17 offset:42752\n"
      "ds_read_b128 v[116:119], v17 offset:42816\n"
      "ds_read_b128 v[120:123], v17 offset:42880\n"
      "ds_read_b128 v[124:127], v17 offset:42944\n"
      "s_waitcnt lgkmcnt(3)\n"
      "v_mfma_f32_16x16x32_bf16 v[8:11], v[64:67], v[112:115], 0\n"
      "v_mfma_f32_16x16x32_bf16 v[12:15], v[68:71], v[112:115], 0\n"
      "s_waitcnt lgkmcnt(2)\n"
      "v_mfma_f32_16x16x32_bf16 v[8:11], v[72:75], v[116:119], v[8:11]\n"
      "v_mfma_f32_16x16x32_bf16 v[12:15], v[76:79], v[116:119], v[12:15]\n"
      "s_waitcnt lgkmcnt(1)\n"
      "v_mfma_f32_16x16x32_bf16 v[8:11], v[80:83], v[120:123], v[8:11]\n"
      "v_mfma_f32_16x16x32_bf16 v[12:15], v[84:87], v[120:123], v[12:15]\n"
      "s_waitcnt lgkmcnt(0)\n"
      "v_mfma_f32_16x16x32_bf16 v[8:11], v[88:91], v[124:127], v[8:11]\n"
      "v_mfma_f32_16x16x32_bf16 v[12:15], v[92:95], v[124:127], v[12:15]\n"
      "s_cmp_eq_u32 s11, 0\n"
      "s_nop 5\n"
      "v_cvt_pk_bf16_f32 v8, v8, v9\n"
      "v_cvt_pk_bf16_f32 v9, v10, v11\n"
      "v_cvt_pk_bf16_f32 v10, v12, v13\n"
      "v_cvt_pk_bf16_f32 v11, v14, v15\n"
      "buffer_store_dwordx4 v[8:11], v4, s[28:31], 0 offen sc1\n"
      "s_cbranch_scc1 .Lfg_done\n"
      "v_subrev_u32 v17, 4352, v17\n"
      "v_subrev_u32 v4, 1048576, v4\n"
      "s_sub_u32 s11, s11, 1\n"
      "s_cmp_eq_u32 s11, 1\n"
      "s_cbranch_scc1 .Lfg_proj\n"
      "s_barrier\n"
      "s_branch .Lfg_proj\n"
      ".Lfg_dob:\n"
      "s_mov_b64 exec, -1\n"
      ".Lfg_pwb:\n"
      "s_waitcnt vmcnt(0)\n"
      "v_xor_b32 v8, s16, v8\n"
      "v_xor_b32 v9, s16, v9\n"
      "v_xor_b32 v10, s16, v10\n"
      "v_xor_b32 v11, s16, v11\n"
      "s_mov_b64 exec, -1\n"
      "v_or3_b32 v6, v8, v9, v10\n"
      "v_lshlrev_b32_sdwa v40, 2, v8 dst_sel:DWORD dst_unused:UNUSED_PAD src0_sel:DWORD src1_sel:BYTE_0\n"
      "v_or_b32 v6, v6, v11\n"
      "v_lshlrev_b32_sdwa v41, 2, v9 dst_sel:DWORD dst_unused:UNUSED_PAD src0_sel:DWORD src1_sel:BYTE_0\n"
      "v_cmp_lt_u32 vcc, s38, v6\n"
      "v_lshlrev_b32_sdwa v42, 2, v10 dst_sel:DWORD dst_unused:UNUSED_PAD src0_sel:DWORD src1_sel:BYTE_0\n"
      "v_lshlrev_b32_sdwa v43, 2, v11 dst_sel:DWORD dst_unused:UNUSED_PAD src0_sel:DWORD src1_sel:BYTE_0\n"
      "s_cbranch_vccnz .Lfg_retryb\n"
      "v_cmp_lt_u32 s[6:7], v40, v21\n"
      "v_cmp_lt_u32 s[8:9], v41, v21\n"
      "v_cmp_lt_u32 s[10:11], v42, v21\n"
      "v_cmp_lt_u32 s[12:13], v43, v21\n"
      "s_mov_b64 exec, s[6:7]\n"
      "ds_add_rtn_u32 v40, v40, v13 offset:0\n"
      "s_mov_b64 exec, s[8:9]\n"
      "ds_add_rtn_u32 v41, v41, v13 offset:0\n"
      "s_mov_b64 exec, s[10:11]\n"
      "ds_add_rtn_u32 v42, v42, v13 offset:0\n"
      "s_mov_b64 exec, s[12:13]\n"
      "ds_add_rtn_u32 v43, v43, v13 offset:0\n"
      "s_mov_b64 exec, s[4:5]\n"
      "v_cmp_ne_u32 vcc, s17, v11\n"
      "s_waitcnt lgkmcnt(0)\n"
      "s_mov_b64 exec, s[12:13]\n"
      "ds_write_b32 v43, v11\n"
      "s_mov_b64 exec, s[10:11]\n"
      "ds_write_b32 v42, v10\n"
      "s_mov_b64 exec, s[8:9]\n"
      "ds_write_b32 v41, v9\n"
      "s_mov_b64 exec, s[6:7]\n"
      "ds_write_b32 v40, v8\n"
      "s_cbranch_vccz .Lfg_puta\n"
      ".Lfg_flag:\n"
      "s_mov_b64 exec, 1\n"
      "v_mov_b32 v6, 160\n"
      "v_mov_b32 v2, -1\n"
      "ds_write_b32 v6, v2\n"
      "s_branch .Lfg_b4x\n"
      ".Lfg_rl:\n"
      "s_cmp_ge_u32 s3, 12\n"
      "s_cbranch_scc1 .Lfg_rlq\n"
      "s_sleep 5\n"
      "global_load_dwordx4 v[64:67], v26, s[12:13] offset:0\n"
      "global_load_dwordx4 v[68:71], v26, s[12:13] offset:1024\n"
      "global_load_dwordx4 v[72:75], v26, s[12:13] offset:2048\n"
      "global_load_dwordx4 v[76:79], v26, s[12:13] offset:3072\n"
      "v_add_u32 v26, 4096, v26\n"
      "global_load_dwordx4 v[80:83], v26, s[12:13] offset:0\n"
      "global_load_dwordx4 v[84:87], v26, s[12:13] offset:1024\n"
      "global_load_dwordx4 v[88:91], v26, s[12:13] offset:2048\n"
      "global_load_dwordx4 v[92:95], v26, s[12:13] offset:3072\n"
      "s_barrier\n"
      "s_waitcnt vmcnt(0)\n"
      "s_lshr_b32 s36, s2, 7\n"
      "s_add_u32 s36, s36, s3\n"
      "s_cmp_lg_u32 s36, 12\n"
      "s_cbranch_scc1 .Lfg_notok\n"
      "s_lshl_b32 s36, s2, 7\n"
      "s_add_u32 s36, s36, 4294955008\n"
      "v_mov_b32 v3, s41\n"
      "s_mov_b64 exec, 1\n"
      "buffer_store_dword v3, off, s[24:27], s36 sc1\n"
      "s_mov_b64 exec, -1\n"
      ".Lfg_notok:\n"
      "ds_write_b128 v16, v[64:67] offset:0\n"
      "ds_write_b128 v16, v[68:71] offset:64\n"
      "ds_write_b128 v16, v[72:75] offset:512\n"
      "ds_write_b128 v16, v[76:79] offset:576\n"
      "ds_write_b128 v16, v[80:83] offset:1024\n"
      "ds_write_b128 v16, v[84:87] offset:1088\n"
      "ds_write_b128 v16, v[88:91] offset:1536\n"
      "ds_write_b128 v16, v[92:95] offset:1600\n"
      "s_setprio 2\n"
      "s_branch .Lfg_go\n"
      ".Lfg_rlq:\n"
      "s_sleep 2\n"
      "buffer_load_dwordx4 v[96:99], v19, s[20:23], 0 offen\n"
      "v_add_u32 v19, 1048576, v19\n"
      "buffer_load_dwordx4 v[100:103], v19, s[20:23], 0 offen\n"
      "v_add_u32 v19, 1048576, v19\n"
      "buffer_load_dwordx4 v[104:107], v19, s[20:23], 0 offen\n"
      "s_setprio 3\n"
      "v_mov_b32 v5, 240\n"
      "s_barrier\n"
      "ds_read_b64_tr_b16 v[64:65], v16 offset:0\n"
      "ds_read_b64_tr_b16 v[66:67], v16 offset:256\n"
      "ds_read_b64_tr_b16 v[68:69], v16 offset:8\n"
      "ds_read_b64_tr_b16 v[70:71], v16 offset:264\n"
      "ds_read_b64_tr_b16 v[72:73], v16 offset:2048\n"
      "ds_read_b64_tr_b16 v[74:75], v16 offset:2304\n"
      "ds_read_b64_tr_b16 v[76:77], v16 offset:2056\n"
      "ds_read_b64_tr_b16 v[78:79], v16 offset:2312\n"
      "ds_read_b64_tr_b16 v[80:81], v16 offset:4096\n"
      "ds_read_b64_tr_b16 v[82:83], v16 offset:4352\n"
      "ds_read_b64_tr_b16 v[84:85], v16 offset:4104\n"
      "ds_read_b64_tr_b16 v[86:87], v16 offset:4360\n"
      "ds_read_b64_tr_b16 v[88:89], v16 offset:6144\n"
      "ds_read_b64_tr_b16 v[90:91], v16 offset:6400\n"
      "ds_read_b64_tr_b16 v[92:93], v16 offset:6152\n"
      "ds_read_b64_tr_b16 v[94:95], v16 offset:6408\n"
      "s_waitcnt vmcnt(0)\n"
      "ds_write_b128 v12, v[96:99] offset:14336\n"
      "ds_write_b128 v12, v[100:103] offset:18688\n"
      "ds_write_b128 v12, v[104:107] offset:23040\n"
      "s_mov_b64 exec, 1\n"
      "ds_add_u32 v5, v13\n"
      "s_mov_b64 exec, -1\n"
      "s_lshl_b32 s36, s40, 6\n"
      "v_add_u32 v19, s36, v17\n"
      ".Lfg_qs:\n"
      "ds_read_b32 v3, v5\n"
      "s_waitcnt lgkmcnt(0)\n"
      "v_readfirstlane_b32 s36, v3\n"
      "s_cmp_lt_u32 s36, 50448\n"
      "s_cbranch_scc1 .Lfg_qs\n"
      "ds_read_b128 v[96:99], v17 offset:23040\n"
      "ds_read_b128 v[100:103], v17 offset:23104\n"
      "ds_read_b128 v[104:107], v17 offset:23168\n"
      "ds_read_b128 v[108:111], v17 offset:23232\n"
      "ds_read_b128 v[112:115], v17 offset:18688\n"
      "ds_read_b128 v[116:119], v17 offset:18752\n"
      "ds_read_b128 v[120:123], v17 offset:18816\n"
      "ds_read_b128 v[124:127], v17 offset:18880\n"
      "ds_read_b128 v[48:51], v17 offset:14336\n"
      "ds_read_b128 v[52:55], v17 offset:14400\n"
      "ds_read_b128 v[56:59], v17 offset:14464\n"
      "ds_read_b128 v[60:63], v17 offset:14528\n"
      "s_waitcnt lgkmcnt(8)\n"
      "v_mfma_f32_16x16x32_bf16 v[32:35], v[64:67], v[96:99], 0\n"
      "v_mfma_f32_16x16x32_bf16 v[36:39], v[68:71], v[96:99], 0\n"
      "v_mfma_f32_16x16x32_bf16 v[32:35], v[72:75], v[100:103], v[32:35]\n"
      "v_mfma_f32_16x16x32_bf16 v[36:39], v[76:79], v[100:103], v[36:39]\n"
      "v_mfma_f32_16x16x32_bf16 v[32:35], v[80:83], v[104:107], v[32:35]\n"
      "v_mfma_f32_16x16x32_bf16 v[36:39], v[84:87], v[104:107], v[36:39]\n"
      "v_mfma_f32_16x16x32_bf16 v[32:35], v[88:91], v[108:111], v[32:35]\n"
      "v_mfma_f32_16x16x32_bf16 v[36:39], v[92:95], v[108:111], v[36:39]\n"
      "s_waitcnt lgkmcnt(4)\n"
      "v_mfma_f32_16x16x32_bf16 v[40:43], v[64:67], v[112:115], 0\n"
      "v_mfma_f32_16x16x32_bf16 v[44:47], v[68:71], v[112:115], 0\n"
      "v_mfma_f32_16x16x32_bf16 v[40:43], v[72:75], v[116:119], v[40:43]\n"
      "v_mfma_f32_16x16x32_bf16 v[44:47], v[76:79], v[116:119], v[44:47]\n"
      "v_mfma_f32_16x16x32_bf16 v[40:43], v[80:83], v[120:123], v[40:43]\n"
      "v_mfma_f32_16x16x32_bf16 v[44:47], v[84:87], v[120:123], v[44:47]\n"
      "v_mfma_f32_16x16x32_bf16 v[40:43], v[88:91], v[124:127], v[40:43]\n"
      "v_mfma_f32_16x16x32_bf16 v[44:47], v[92:95], v[124:127], v[44:47]\n"
      "s_waitcnt lgkmcnt(0)\n"
      "v_mfma_f32_16x16x32_bf16 v[8:11], v[64:67], v[48:51], 0\n"
      "v_mfma_f32_16x16x32_bf16 v[96:99], v[68:71], v[48:51], 0\n"
      "v_mfma_f32_16x16x32_bf16 v[8:11], v[72:75], v[52:55], v[8:11]\n"
      "v_mfma_f32_16x16x32_bf16 v[96:99], v[76:79], v[52:55], v[96:99]\n"
      "v_mfma_f32_16x16x32_bf16 v[8:11], v[80:83], v[56:59], v[8:11]\n"
      "v_mfma_f32_16x16x32_bf16 v[96:99], v[84:87], v[56:59], v[96:99]\n"
      "v_mfma_f32_16x16x32_bf16 v[8:11], v[88:91], v[60:63], v[8:11]\n"
      "v_mfma_f32_16x16x32_bf16 v[96:99], v[92:95], v[60:63], v[96:99]\n"
      "v_cvt_pk_bf16_f32 v32, v32, v33\n"
      "v_cvt_pk_bf16_f32 v33, v34, v35\n"
      "v_cvt_pk_bf16_f32 v34, v36, v37\n"
      "v_cvt_pk_bf16_f32 v35, v38, v39\n"
      "ds_write_b128 v19, v[32:35] offset:9984\n"
      "v_cvt_pk_bf16_f32 v40, v40, v41\n"
      "v_cvt_pk_bf16_f32 v41, v42, v43\n"
      "v_cvt_pk_bf16_f32 v42, v44, v45\n"
      "v_cvt_pk_bf16_f32 v43, v46, v47\n"
      "ds_write_b128 v19, v[40:43] offset:5632\n"
      "v_cvt_pk_bf16_f32 v8, v8, v9\n"
      "v_cvt_pk_bf16_f32 v9, v10, v11\n"
      "v_cvt_pk_bf16_f32 v10, v96, v97\n"
      "v_cvt_pk_bf16_f32 v11, v98, v99\n"
      "ds_write_b128 v19, v[8:11] offset:1280\n"
      "s_mov_b64 exec, 1\n"
      "ds_add_u32 v5, v13\n"
      "s_mov_b64 exec, -1\n"
      "v_mov_b64 v[48:49], 0\n"
      "v_mov_b64 v[50:51], 0\n"
      "v_mov_b64 v[52:53], 0\n"
      "v_mov_b64 v[54:55], 0\n"
      "v_mov_b64 v[56:57], 0\n"
      "v_mov_b64 v[58:59], 0\n"
      "v_mov_b64 v[60:61], 0\n"
      "v_mov_b64 v[62:63], 0\n"
      "v_mov_b64 v[32:33], 0\n"
      "v_mov_b64 v[34:35], 0\n"
      "v_mov_b64 v[36:37], 0\n"
      "v_mov_b64 v[38:39], 0\n"
      "s_setprio 2\n"
      "s_branch .Lfg_go\n"
      ".Lfg_rare:\n"
      "s_cmp_eq_u32 s10, 0\n"
      "s_cbranch_scc1 .Lfg_qrd\n"
      "v_mov_b32 v5, 240\n"
      "s_mov_b32 s10, 0\n"
      ".Lfg_qw:\n"
      "ds_read_b32 v3, v5\n"
      "s_waitcnt lgkmcnt(0)\n"
      "v_readfirstlane_b32 s36, v3\n"
      "s_cmp_lt_u32 s36, 50464\n"
      "s_cbranch_scc0 .Lfg_qok\n"
      "s_sleep 2\n"
      "s_branch .Lfg_qw\n"
      ".Lfg_qok:\n"
      "s_cmp_eq_u32 s44, 0\n"
      "s_cbranch_scc1 .Lfg_tick\n"
      "s_branch .Lfg_qrd\n"
      ".Lfg_np:\n"
      "s_waitcnt lgkmcnt(0)\n"
      "s_add_u32 s41, s33, 0x400000\n"
      "s_and_b32 s33, s41, 0xffc00000\n"
      "s_add_u32 s19, s18, 120\n"
      "s_branch .Lfg_dlt\n"
      ".Lfg_dlts:\n"
      "s_sleep 1\n"
      ".Lfg_dlt:\n"
      "s_memrealtime s[36:37]\n"
      "s_waitcnt lgkmcnt(0)\n"
      "s_sub_u32 s36, s36, s19\n"
      "s_cmp_lt_i32 s36, 0\n"
      "s_cbranch_scc1 .Lfg_dlts\n"
      "s_load_dwordx8 s[4:11], s[14:15], 2048\n"
      "s_cmp_lt_u32 s3, 12\n"
      "s_cbranch_scc1 .Lfg_consts\n"
      "s_waitcnt vmcnt(0)\n"
      "s_branch .Lfg_consts\n"
      ".Lfg_tok:\n"
      "s_barrier\n"
      "s_memrealtime s[44:45]\n"
      "s_lshl_b32 s38, s2, 2\n"
      "v_mov_b32 v3, s38\n"
      "s_waitcnt lgkmcnt(0)\n"
      "v_mov_b32 v2, s44\n"
      "s_mov_b64 exec, 1\n"
      "global_store_dword v3, v2, s[14:15] offset:1568 sc1\n"
      "s_mov_b64 exec, -1\n"
      "s_load_dword s33, s[14:15], 0\n"
      "s_branch .Lfg_b1\n"
      ".Lfg_tokr:\n"
      "s_add_u32 s42, s42, 1\n"
      "s_cmp_gt_u32 s42, 200\n"
      "s_cbranch_scc1 .Lfg_toke\n"
      "s_sleep 4\n"
      "v_and_b32 v2, 28, v15\n"
      "global_load_dword v2, v2, s[14:15] offset:2048 sc1\n"
      "s_waitcnt vmcnt(0)\n"
      "v_readlane_b32 s4, v2, 0\n"
      "v_readlane_b32 s5, v2, 1\n"
      "v_readlane_b32 s6, v2, 2\n"
      "v_readlane_b32 s7, v2, 3\n"
      "v_readlane_b32 s8, v2, 4\n"
      "v_readlane_b32 s9, v2, 5\n"
      "v_readlane_b32 s10, v2, 6\n"
      "v_readlane_b32 s11, v2, 7\n"
      "s_branch .Lfg_tokc\n"
      ".Lfg_sp2:\n"
      "v_add_u32 v112, 16, v112\n"
      "v_add_u32 v115, 16, v115\n"
      "v_subrev_u32 v119, 16, v119\n"
      "ds_read_b128 v[120:123], v112\n"
      "s_branch .Lfg_sp\n"
      ".Lfg_retry:\n"
      "s_add_u32 s42, s42, 1\n"
      "s_cmp_gt_u32 s42, 0x40000\n"
      "s_cbranch_scc1 .Lfg_flag\n"
      "s_mov_b64 exec, vcc\n"
      "s_sleep 4\n"
      "buffer_load_dwordx4 v[2:5], v18, s[24:27], s39 offen sc1\n"
      "s_waitcnt vmcnt(0)\n"
      "s_branch .Lfg_chk\n"
      ".Lfg_retryb:\n"
      "s_add_u32 s42, s42, 1\n"
      "s_cmp_gt_u32 s42, 0x40000\n"
      "s_cbranch_scc1 .Lfg_flag\n"
      "s_mov_b64 exec, vcc\n"
      "s_sleep 4\n"
      "buffer_load_dwordx4 v[8:11], v18, s[24:27], s49 offen sc1\n"
      "s_branch .Lfg_pwb\n"
      ".Lfg_cont:\n"
      "v_add_u32 v20, 256, v20\n"
      "s_branch .Lfg_ent\n"
      ".Lfg_sig4:\n"
      "v_add_f32_dpp v8, v9, v8 quad_perm:[1,1,1,1] row_mask:0xf bank_mask:0xf\n"
      "v_add_f32_dpp v10, v11, v10 quad_perm:[3,3,3,3] row_mask:0xf bank_mask:0xf\n"
      "s_nop 1\n"
      "v_add_f32_dpp v8, v10, v8 quad_perm:[2,2,2,2] row_mask:0xf bank_mask:0xf\n"
      "s_nop 1\n"
      "v_mul_f32_dpp v8, v8, v23 quad_perm:[0,0,0,0] row_mask:0xf bank_mask:0xf\n"
      "v_exp_f32 v8, v8\n"
      "s_nop 0\n"
      "v_add_f32 v8, 1.0, v8\n"
      "v_rcp_f32 v8, v8\n"
      "s_nop 0\n"
      "v_cvt_pk_bf16_f32 v8, v8, v8\n"
      "v_and_b32 v10, v8, v24\n"
      "v_and_b32 v11, v8, v25\n"
      "s_branch .Lfg_sig3\n"
      ".Lfg_slow:\n"
      "s_setprio 0\n"
      "s_mov_b32 s40, 0\n"
      "s_cmp_lg_u32 s3, 0\n"
      "s_cbranch_scc1 .Lfg_end\n"
      ".Lfg_bump:\n"
      "s_cmp_lg_u32 s2, 255\n"
      "s_cbranch_scc1 .Lfg_bx\n"
      "v_lshlrev_b32 v18, 8, v1\n"
      "s_mov_b32 s42, 0\n"
      "s_movk_i32 s36, 4096\n"
      ".Lfg_bp:\n"
      "buffer_load_dword v2, v18, s[24:27], s36 offen sc1\n"
      "buffer_load_dword v3, v18, s[24:27], s36 offen offset:128 sc1\n"
      "s_add_u32 s42, s42, 1\n"
      "s_cmp_gt_u32 s42, 0x40000\n"
      "s_waitcnt vmcnt(0)\n"
      "s_cbranch_scc1 .Lfg_bq\n"
      "v_xor_b32 v2, s41, v2\n"
      "v_xor_b32 v3, s41, v3\n"
      "v_or_b32 v2, v2, v3\n"
      "v_cmp_ne_u32 vcc, 0, v2\n"
      "s_nop 0\n"
      "s_cbranch_vccnz .Lfg_bp\n"
      ".Lfg_bq:\n"
      "v_mov_b32 v2, s41\n"
      "s_mov_b64 exec, 1\n"
      "buffer_store_dword v2, off, s[24:27], 0 sc1\n"
      "s_mov_b64 exec, -1\n"
      ".Lfg_bx:\n"
      "s_cmp_eq_u32 s40, 4\n"
      "s_cbranch_scc1 .Lfg_fin\n"
      ".Lfg_end:\n"
      "s_waitcnt vmcnt(0) lgkmcnt(0)\n"
      : "+{s[0:1]}"(ka)
      : "{s2}"(blockIdx.x), "{v0}"(threadIdx.x)
      : "v1", "v2", "v3", "v4", "v5", "v6", "v7", "v8", "v9", "v10", "v11", "v12", "v13", "v14", "v15", "v16", "v17", "v18", "v19", "v20", "v21", "v22", "v23", "v24", "v25", "v26", "v27", "v28", "v29", "v30", "v31", "v32", "v33", "v34", "v35", "v36", "v37", "v38", "v39", "v40", "v41", "v42", "v43", "v44", "v45", "v46", "v47", "v48", "v49", "v50", "v51", "v52", "v53", "v54", "v55", "v56", "v57", "v58", "v59", "v60", "v61", "v62", "v63", "v64", "v65", "v66", "v67", "v68", "v69", "v70", "v71", "v72", "v73", "v74", "v75", "v76", "v77", "v78", "v79", "v80", "v81", "v82", "v83", "v84", "v85", "v86", "v87", "v88", "v89", "v90", "v91", "v92", "v93", "v94", "v95", "v96", "v97", "v98", "v99", "v100", "v101", "v102", "v103", "v104", "v105", "v106", "v107", "v108", "v109", "v110", "v111", "v112", "v113", "v114", "v115", "v116", "v117", "v118", "v119", "v120", "v121", "v122", "v123", "v124", "v125", "v126", "v127", "s3", "s4", "s5", "s6", "s7", "s8", "s9", "s10", "s11", "s12", "s13", "s14", "s15", "s16", "s17", "s18", "s19", "s20", "s21", "s22", "s23", "s24", "s25", "s26", "s27", "s28", "s29", "s30", "s31", "s33", "s34", "s35", "s36", "s37", "s38", "s39", "s40", "s41", "s42", "s43", "s44", "s45", "s46", "s47", "s48", "s49", "s50", "s51", "s52", "s53", "s54", "s55", "s56", "s57", "s58", "s59", "s60", "s61", "s62", "s63", "vcc", "scc", "memory");
  typedef __attribute__((address_space(1))) char gchar;
  const gchar* x = (const gchar*)ka[0];
  const gint* src = (const gint*)ka[1];
  const gint4* dst = (const gint4*)ka[2];
  gchar* out = (gchar*)ka[6];

  char LAS* lds = (char LAS*)smem;
  const int tid = threadIdx.x, lane = tid & 63, b = (blockIdx.x >> 3) | ((blockIdx.x & 7) << 5);
  const int w = __builtin_amdgcn_readfirstlane(tid >> 6);
  const float kneg = -0.08838834764831845f * 1.4426950408889634f;
  float LAS* ag = (float LAS*)(lds + L_WQ);
  u32 LAS* qu = (u32 LAS*)(lds + L_XD);
  u32 LAS* qn = (u32 LAS*)(lds + L_CNT);
  __syncthreads();
  for (int i = tid; i < NPB * DD; i += 1024) ag[i] = 0.f;
  for (int base = 0; base < NE; base += 4096) {
    __syncthreads();
    if (tid == 0) *qn = 0;
    __syncthreads();
    const int i = base / 4 + tid;
    if (i < NE / 4) {
      const i32x4 d4 = dst[i];
#pragma unroll
      for (int j = 0; j < 4; ++j)
        if ((d4[j] & 255) == b) {
          const u32 p = __hip_atomic_fetch_add(qn, 1u, __ATOMIC_RELAXED, __HIP_MEMORY_SCOPE_WORKGROUP);
          qu[p] = ((u32)src[4 * i + j] << 8) | ((u32)d4[j] >> 8);
        }
    }
    __syncthreads();
    const int n = *qn;
    for (int k = w; k < n; k += 16) {
      const u32 rec = qu[k];
      const int nd = rec & 0xFF;
      const u32 xv = *(const GAS u32*)(x + (rec & ~0xFFu) + lane * 4);
      const u32 qv = *(const u32 LAS*)(lds + L_QT + nd * PITCH + lane * 4);
      const float x0 = __builtin_bit_cast(float, xv << 16), x1 = __builtin_bit_cast(float, xv & 0xFFFF0000u);
      float t = x0 * __builtin_bit_cast(float, qv << 16) + x1 * __builtin_bit_cast(float, qv & 0xFFFF0000u);
#pragma unroll
      for (int o = 32; o > 0; o >>= 1) t += __shfl_xor(t, o);
      const float al0 = __builtin_amdgcn_rcpf(1.0f + __builtin_amdgcn_exp2f(t * kneg));
      const float al = __builtin_bit_cast(float, pk_bf16(al0, al0) << 16);
      __hip_atomic_fetch_add(ag + nd * DD + 2 * lane, al * x0, __ATOMIC_RELAXED, __HIP_MEMORY_SCOPE_WORKGROUP);
      __hip_atomic_fetch_add(ag + nd * DD + 2 * lane + 1, al * x1, __ATOMIC_RELAXED, __HIP_MEMORY_SCOPE_WORKGROUP);
    }
  }
  __syncthreads();
  for (int i = tid; i < NPB * 64; i += 1024) {
    const int f = (i >> 6) * DD + ((i & 63) >> 2) * 8 + (i & 3);
    *(u32 LAS*)(lds + L_AG + (i >> 6) * PITCH + (i & 63) * 4) = pk_bf16(ag[f], ag[f + 4]);
  }
  __syncthreads();
  if (w < 12) {
    const int mt = w >> 2, cb = w & 3, g = lane >> 4;
    const int wa = L_WV + cb * 8256 + g * 512 + (lane & 15) * 16;
    const int ra = L_AG + (mt * 16 + (lane & 15)) * PITCH + g * 16;
    f32x4 o0 = {0, 0, 0, 0}, o1 = o0;
#pragma unroll
    for (int s = 0; s < 4; ++s) {
      const u32x4 bv = *(const u32x4 LAS*)(lds + ra + s * 64);
      o0 = mfma32(tr_read(lds + wa + s * 2048), tr_read(lds + wa + s * 2048 + 256), bv, o0);
      o1 = mfma32(tr_read(lds + wa + s * 2048 + 8), tr_read(lds + wa + s * 2048 + 264), bv, o1);
    }
    const int node = (mt * 16 + (lane & 15)) * NB + b;
    if (node < NN)
      *(GAS u32x4*)(out + (size_t)node * 256 + (4 * cb + g) * 16) =
          (u32x4){pk_bf16(o0.x, o0.y), pk_bf16(o0.z, o0.w), pk_bf16(o1.x, o1.y), pk_bf16(o1.z, o1.w)};
  }
}

extern "C" void kernel_launch(void* const* d_in, const int* in_sizes, int n_in, void* d_out, int out_size, void* d_ws,
                              size_t ws_size, hipStream_t stream) {
  featgraph_kernel<<<NB, 1024, 0, stream>>>((const char*)d_in[0], (const int*)d_in[1], (const int*)d_in[2], (const char*)d_in[3],
                                            (const char*)d_in[4], (char*)d_ws + WS_OFF, (char*)d_out);
}
```
